```python
import torch

def _softmax(x, axis=-1):
    return torch.softmax(x, dim=axis)
def _gelu(x):
    return torch.nn.functional.gelu(x, approximate="tanh")
def _silu(x):
    return torch.nn.functional.silu(x)
def _sigmoid(x):
    return torch.sigmoid(x)
def _layernorm(x, axis=-1, eps=1e-5):
    m = x.mean(dim=axis, keepdim=True)
    v = x.var(dim=axis, keepdim=True, unbiased=False)
    return (x - m) / torch.sqrt(v + eps)
def _scatter_add(tgt, idx, src, axis=0):
    return tgt.clone().index_add_(axis, idx, src)
def _segment_sum(src, seg, n):
    out = torch.zeros((n,) + src.shape[1:], dtype=src.dtype)
    return out.index_add_(0, seg, src)
def _pad(x, pw):
    flat = tuple(v for pair in reversed(pw) for v in pair)
    return torch.nn.functional.pad(x, flat)

kernel_name = 'sequential_preLN__ssm_conv_s4__dense_activation_relu__prompt'

def setup_inputs():

    _w = (lambda a: (lambda t: t.to(torch.bfloat16) if t.is_floating_point() else t)(torch.as_tensor(a)))
    def _A_setup():
        B, L, D, N = 1, 2048, 448, 256
        A_re = -(lambda _x: torch.exp(torch.as_tensor(_x)))((lambda *s: torch.randn(*s, dtype=torch.bfloat16) * 3.2)(D, N))
        A_im = torch.pi * (lambda *s: torch.randn(*s, dtype=torch.bfloat16) * 0.05)(D, N)
        B_re = (lambda *s: torch.randn(*s, dtype=torch.bfloat16) * 0.05)(D, N)
        B_im = (lambda *s: torch.randn(*s, dtype=torch.bfloat16) * 0.05)(D, N)
        C_re = (lambda *s: torch.randn(*s, dtype=torch.bfloat16) * 0.05)(D, N)
        C_im = (lambda *s: torch.randn(*s, dtype=torch.bfloat16) * 0.05)(D, N)
        log_dt = (lambda *s: torch.randn(*s, dtype=torch.bfloat16) * 0.05)(D) * 0.5 - 4.0
        Dskip = (lambda *s: torch.randn(*s, dtype=torch.bfloat16) * 0.05)(D)
        return {
            "u": _w((lambda *s: torch.randn(*s, dtype=torch.bfloat16) * 0.05)(B, L, D)),
            "A_re": _w(A_re),
            "A_im": _w(A_im),
            "B_re": _w(B_re),
            "B_im": _w(B_im),
            "C_re": _w(C_re),
            "C_im": _w(C_im),
            "log_dt": _w(log_dt),
            "Dskip": _w(Dskip),
        }
    _A_in = _A_setup()

    def _B_setup():
        B, S, D, Dff = 1, 2048, 448, 1792
        return {
            "x": _w((lambda *s: torch.randn(*s, dtype=torch.bfloat16) * 0.05)(B, S, D)),
            "w1": _w((lambda *s: torch.randn(*s, dtype=torch.bfloat16) * 0.05)(D, Dff)),
            "w2": _w((lambda *s: torch.randn(*s, dtype=torch.bfloat16) * 0.05)(Dff, D)),
        }
    _B_in = _B_setup()

    return {
        "A_u": _A_in["u"],
        "A_A_re": _A_in["A_re"],
        "A_A_im": _A_in["A_im"],
        "A_B_re": _A_in["B_re"],
        "A_B_im": _A_in["B_im"],
        "A_C_re": _A_in["C_re"],
        "A_C_im": _A_in["C_im"],
        "A_log_dt": _A_in["log_dt"],
        "A_Dskip": _A_in["Dskip"],
        "B_x": _B_in["x"],
        "B_w1": _B_in["w1"],
        "B_w2": _B_in["w2"],
    }

def reference(A_u, A_A_re, A_A_im, A_B_re, A_B_im, A_C_re, A_C_im, A_log_dt, A_Dskip, B_x, B_w1, B_w2):
    def _A_ref(u, A_re, A_im, B_re, B_im, C_re, C_im, log_dt, Dskip):
        Bsz, L, D = u.shape
        dt = torch.exp(log_dt)[:, None]
        A = A_re + 1j * A_im
        Bc = B_re + 1j * B_im
        Cc = C_re + 1j * C_im
        logdA = dt * A
        dB = dt * Bc
        l = (lambda x, dt: x.to(dt))(torch.arange(L), A_re.dtype)
        P = torch.exp(logdA[:, :, None] * l[None, None, :])
        K = torch.einsum("dn,dnl,dn->dl", Cc, P, dB).real
        L2 = 2 * L
        Kp = _pad(K, ((0, 0), (0, L)))
        up = (lambda x, *d: x.permute(*d))(u, 0, 2, 1)
        up = _pad(up, ((0, 0), (0, 0), (0, L)))
        Kf = (lambda x, axes=(-2,-1): torch.fft.rfft2(x.float(), dim=axes))(Kp[:, None, :], axes=(-2, -1))
        Uf = (lambda x, axes=(-2,-1): torch.fft.rfft2(x.float(), dim=axes))(up[:, :, None, :], axes=(-2, -1))
        Yf = Uf * Kf[None, :, :, :]
        y = (lambda x, s, axes=(-2,-1): torch.fft.irfft2(x, s=s, dim=axes).to(torch.bfloat16))(Yf, (1, L2), axes=(-2, -1))[:, :, 0, :L]
        y = (lambda x, *d: x.permute(*d))(y, 0, 2, 1) + u * Dskip[None, None, :]
        return y

    def _B_ref(x, w1, w2):
        h = torch.einsum("bsd,df->bsf", x, w1)
        zero = torch.zeros_like(h)
        h = torch.maximum(h, zero)
        return torch.einsum("bsf,fd->bsd", h, w2)

    x = A_u
    h = x + _A_ref(_layernorm(x, axis=-1), A_A_re, A_A_im, A_B_re, A_B_im, A_C_re, A_C_im, A_log_dt, A_Dskip)
    h2 = h + _B_ref(_layernorm(h, axis=-1), B_w1, B_w2)
    return h2
```

```cpp
#include <hip/hip_runtime.h>
#include <stdint.h>

namespace {

typedef __attribute__((ext_vector_type(8))) _Float16 f16x8;
typedef __attribute__((ext_vector_type(4))) _Float16 f16x4;
typedef __attribute__((ext_vector_type(2))) _Float16 f16x2;
typedef __attribute__((ext_vector_type(8))) __bf16 bf16x8;
typedef __attribute__((ext_vector_type(2))) __bf16 bf16x2;
typedef __attribute__((ext_vector_type(4))) float f32x4;
typedef __attribute__((ext_vector_type(16))) float f32x16;
typedef __attribute__((ext_vector_type(4))) unsigned u32x4;
typedef __attribute__((ext_vector_type(2))) unsigned u32x2;
typedef __attribute__((ext_vector_type(4))) short s16x4;
typedef unsigned long long u64;
#define LAS __attribute__((address_space(3)))
#define GAS __attribute__((address_space(1)))
#define DI __device__ __forceinline__

constexpr int L = 2048, D = 448, NM = 256, F = 1792;

constexpr size_t WS_EPOCH = 0;
constexpr size_t WS_TMO = 64;
constexpr size_t WS_FLAGA = 4096;
constexpr size_t WS_FLAGB = 8192;
constexpr size_t WS_UX = 16384;
constexpr size_t WS_HT = WS_UX + (size_t)D * 256 * 32;
constexpr size_t WS_W1F = WS_HT + (size_t)D * L * 2;
constexpr size_t WS_W2F = WS_W1F + (size_t)D * F * 2;
constexpr size_t WS_H1F = WS_W2F + (size_t)D * F * 2;
constexpr size_t WS_END = WS_H1F + (size_t)L * F * 2;
constexpr size_t WS_STAMP = WS_END;
constexpr unsigned kSpinCap = 1u << 20;

DI float bf2f(unsigned b) { return __uint_as_float(b << 16); }
DI float bflo(unsigned p) { return __uint_as_float(p << 16); }
DI float bfhi(unsigned p) { return __uint_as_float(p & 0xffff0000u); }
DI unsigned pkbf(float a, float b) { bf16x2 r; r[0] = (__bf16)a; r[1] = (__bf16)b; return __builtin_bit_cast(unsigned, r); }
DI float rbf(float a) { return bflo(pkbf(a, a)); }
DI unsigned pkh(float a, float b) { f16x2 r; r[0] = (_Float16)a; r[1] = (_Float16)b; return __builtin_bit_cast(unsigned, r); }

template <int CTRL> DI float dppf(float v) { return __int_as_float(__builtin_amdgcn_update_dpp(0, __float_as_int(v), CTRL, 0xF, 0xF, true)); }
DI float rdl(float v, int l) { return __int_as_float(__builtin_amdgcn_readlane(__float_as_int(v), l)); }
DI float wave_sum(float v) {
  v += dppf<0xB1>(v); v += dppf<0x4E>(v); v += dppf<0x141>(v); v += dppf<0x140>(v);
  return (rdl(v, 0) + rdl(v, 16)) + (rdl(v, 32) + rdl(v, 48));
}
DI float wave_max(float v) {
  v = fmaxf(v, dppf<0xB1>(v)); v = fmaxf(v, dppf<0x4E>(v)); v = fmaxf(v, dppf<0x141>(v)); v = fmaxf(v, dppf<0x140>(v));
  return fmaxf(fmaxf(rdl(v, 0), rdl(v, 16)), fmaxf(rdl(v, 32), rdl(v, 48)));
}
DI s16x4 tr_read(const LAS char* p) { return __builtin_amdgcn_ds_read_tr16_b64_v4i16((LAS s16x4*)p); }
DI u32x4 tr_read2(const LAS char* p0, const LAS char* p1) {
  s16x4 a = tr_read(p0), b = tr_read(p1);
  u32x2 a2 = __builtin_bit_cast(u32x2, a), b2 = __builtin_bit_cast(u32x2, b);
  u32x4 r; r[0] = a2[0]; r[1] = a2[1]; r[2] = b2[0]; r[3] = b2[1]; return r;
}
DI f32x4 mfma16h(u32x4 a, u32x4 b, f32x4 c) { return __builtin_amdgcn_mfma_f32_16x16x32_f16(__builtin_bit_cast(f16x8, a), __builtin_bit_cast(f16x8, b), c, 0, 0, 0); }
DI f32x16 mfma32b(u32x4 a, u32x4 b, f32x16 c) { return __builtin_amdgcn_mfma_f32_32x32x16_bf16(__builtin_bit_cast(bf16x8, a), __builtin_bit_cast(bf16x8, b), c, 0, 0, 0); }
DI float dot2bf(unsigned a, unsigned b, float c) { return __builtin_amdgcn_fdot2_f32_bf16(__builtin_bit_cast(bf16x2, a), __builtin_bit_cast(bf16x2, b), c, false); }

struct cf { float r, i; };
DI cf cmul(cf a, cf b) { cf o; o.r = a.r * b.r - a.i * b.i; o.i = a.r * b.i + a.i * b.r; return o; }
DI cf csq(cf a) { cf o; o.r = a.r * a.r - a.i * a.i; o.i = 2.f * a.r * a.i; return o; }
DI cf cexp_p2(float zr2, float zf, float p2) {
  float m = __builtin_amdgcn_exp2f(zr2 * p2);
  float t = __builtin_amdgcn_fractf(zf * p2);
  cf o; o.r = m * __builtin_amdgcn_cosf(t); o.i = m * __builtin_amdgcn_sinf(t); return o;
}

DI void ln_scalars(float mean, float var, float& mb, float& rinv) {
  mb = rbf(mean);
  float vb = rbf(var);
  float sb = rbf(__builtin_sqrtf(rbf(vb + 1e-5f)));
  rinv = 1.0f / sb;
}

constexpr int A_T = 0;
constexpr int A_TAB = 14336;
constexpr int A_MISC = A_TAB + 8 * 16384;
constexpr int A_KDC = A_MISC;
constexpr int A_SC = A_KDC + 2 * 1312;
constexpr int A_LDS = A_SC + 128;
constexpr int A_TOE = A_TAB + 32768;
constexpr int TOE_CP = 4160, TOE_CH = 4 * TOE_CP;
constexpr int A_U16 = A_TAB;
constexpr int A_TOE2 = A_TAB + 9216;
constexpr int A_YP = A_TAB + 42496;

__global__ __launch_bounds__(512, 2) void k_ssm(const unsigned short* __restrict__ x, const unsigned short* __restrict__ Are, const unsigned short* __restrict__ Aim,
                                                 const unsigned short* __restrict__ Bre, const unsigned short* __restrict__ Bim, const unsigned short* __restrict__ Cre,
                                                 const unsigned short* __restrict__ Cim, const unsigned short* __restrict__ logdt, const unsigned short* __restrict__ Dsk,
                                                 const unsigned short* __restrict__ w1, const unsigned short* __restrict__ w2, char* __restrict__ ws) {
  __shared__ __attribute__((aligned(16))) char smem_[A_LDS];
  LAS char* smem = (LAS char*)smem_;
  const int tid = threadIdx.x, lane = tid & 63;
  const int w = __builtin_amdgcn_readfirstlane(tid >> 6);
  const int b = blockIdx.x;
  const bool has_ch = b < 224;
  const int c2 = w >> 2, w4 = w & 3;
  const int ch = 2 * b + c2;

  const u64 E = __hip_atomic_load((GAS u64*)(ws + WS_EPOCH), __ATOMIC_RELAXED, __HIP_MEMORY_SCOPE_AGENT);
  const int row = 8 * b + w;
  u32x4 xv = {0u, 0u, 0u, 0u};
  if (lane < 56) xv = *(const u32x4*)(x + (size_t)row * D + lane * 8);
  const int gw = b * 8 + w;
  const bool has_rt = gw < 1568;
  const bool is_w1 = gw < 784;
  u32x4 rt0 = {0u, 0u, 0u, 0u}, rt1 = {0u, 0u, 0u, 0u};
  int rt_ks = 0, rt_nb = 0;
  if (has_rt) {
    const unsigned short* src; int ld;
    if (is_w1) { rt_ks = gw / 28; rt_nb = gw % 28; ld = F; src = w1 + (size_t)(16 * rt_ks) * F + 64 * rt_nb; }
    else { int g2 = gw - 784; rt_ks = g2 / 7; rt_nb = g2 % 7; ld = D; src = w2 + (size_t)(16 * rt_ks) * D + 64 * rt_nb; }
    rt0 = *(const u32x4*)(src + (size_t)(lane >> 3) * ld + (lane & 7) * 8);
    rt1 = *(const u32x4*)(src + (size_t)(8 + (lane >> 3)) * ld + (lane & 7) * 8);
  }
  float are = 0.f, aim = 0.f, bre = 0.f, bim = 0.f, cre = 0.f, cim = 0.f, ldt = 0.f, dsk = 0.f;
  if (has_ch) {
    const int o = ch * NM + w4 * 64 + lane;
    are = bf2f(Are[o]); aim = bf2f(Aim[o]); bre = bf2f(Bre[o]); bim = bf2f(Bim[o]); cre = bf2f(Cre[o]); cim = bf2f(Cim[o]);
    ldt = bf2f(logdt[ch]); dsk = bf2f(Dsk[ch]);
  }
  if (tid == 0) *(LAS unsigned*)(smem + A_SC + 64) = 0u;

  {
    float xf[8];
#pragma unroll
    for (int j = 0; j < 4; ++j) { xf[2 * j] = bflo(xv[j]); xf[2 * j + 1] = bfhi(xv[j]); }
    float s = 0.f;
#pragma unroll
    for (int j = 0; j < 8; ++j) s += xf[j];
    const float mean = wave_sum(s) * (1.0f / D);
    float q = 0.f;
#pragma unroll
    for (int j = 0; j < 8; ++j) { float d = xf[j] - mean; q += d * d; }
    if (lane >= 56) q = 0.f;
    const float var = wave_sum(q) * (1.0f / D);
    float mb, rinv; ln_scalars(mean, var, mb, rinv);
    u32x4 uv;
#pragma unroll
    for (int j = 0; j < 4; ++j) {
      unsigned t = pkbf(xf[2 * j] - mb, xf[2 * j + 1] - mb);
      uv[j] = pkbf(bflo(t) * rinv, bfhi(t) * rinv);
    }
    if (lane < 56) {
      *(LAS u32x4*)(smem + A_T + w * 896 + lane * 16) = uv;
      *(LAS u32x4*)(smem + A_T + 7168 + w * 896 + lane * 16) = xv;
    }
  }
  __syncthreads();
  if (w < 7) {
    const int g = lane >> 4, q = (lane & 15) >> 2, p = lane & 3;
    const LAS char* a0 = smem + A_T + q * 896 + (64 * w + 16 * g + 4 * p) * 2;
    u32x4 uu = tr_read2(a0, a0 + 4 * 896);
    u32x4 xx = tr_read2(a0 + 7168, a0 + 7168 + 4 * 896);
    const int d = 64 * w + lane;
    auto rs = __builtin_amdgcn_make_buffer_rsrc((void*)(ws + WS_UX), 0, D * 256 * 32, 0x00020000);
    __builtin_amdgcn_raw_buffer_store_b128(uu, rs, (d * 256 + b) * 32, 0, 16);
    __builtin_amdgcn_raw_buffer_store_b128(xx, rs, (d * 256 + b) * 32 + 16, 0, 16);
  }

  LAS char* tab = smem + A_TAB + w * 16384;
  if (has_rt) {
    *(LAS u32x4*)(tab + lane * 16) = rt0;
    *(LAS u32x4*)(tab + 1024 + lane * 16) = rt1;
    __builtin_amdgcn_s_waitcnt(0xC07F);
    __builtin_amdgcn_wave_barrier();
    const int g = lane >> 4, blk = g & 1, hh = g >> 1, q = (lane & 15) >> 2, p = lane & 3;
    const int r0 = is_w1 ? (8 * hh + q) : (4 * hh + q);
    const int r1 = is_w1 ? (8 * hh + 4 + q) : (8 + 4 * hh + q);
#pragma unroll
    for (int t = 0; t < 2; ++t) {
      const int cb = (t * 32 + blk * 16 + 4 * p) * 2;
      u32x4 fr = tr_read2(tab + r0 * 128 + cb, tab + r1 * 128 + cb);
      char* dst = is_w1 ? (ws + WS_W1F + ((size_t)(2 * rt_nb + t) * 28 + rt_ks) * 1024) : (ws + WS_W2F + ((size_t)(2 * rt_nb + t) * 112 + rt_ks) * 1024);
      *(u32x4*)(dst + lane * 16) = fr;
    }
    __builtin_amdgcn_s_waitcnt(0xC07F);
    __builtin_amdgcn_wave_barrier();
  }

  const u64 tag = E + 1;
  auto publish_flag = [&]() {
    asm volatile("s_waitcnt vmcnt(0)" ::: "memory");
    unsigned old = 0;
    if (lane == 0) old = __hip_atomic_fetch_add((LAS unsigned*)(smem + A_SC + 64), 1u, __ATOMIC_RELAXED, __HIP_MEMORY_SCOPE_WORKGROUP);
    old = __builtin_amdgcn_readfirstlane(old);
    if (old == 7u && lane == 0) __hip_atomic_store((GAS u64*)(ws + WS_FLAGA) + b, tag, __ATOMIC_RELAXED, __HIP_MEMORY_SCOPE_AGENT);
  };
  if (!has_ch) { publish_flag(); return; }

  float k2048;
  int ew;
  {
    const float dt = rbf(expf(ldt));
    const float zr = dt * are, zi = dt * aim;
    const float zr2 = zr * 1.44269504088896341f, zf = zi * 0.15915494309189535f;
    const float dbr = dt * bre, dbi = dt * bim;
    cf CB; CB.r = cre * dbr - cim * dbi; CB.i = cre * dbi + cim * dbr;
    const float mx = wave_max(fmaxf(fabsf(CB.r), fabsf(CB.i)));
    const float gz = fmaxf(wave_max(zr2), 0.f) * 2048.f;
    int e = (mx > 0.f) ? ((int)(__float_as_uint(mx) >> 23) - 127) : 0;
    e += (int)ceilf(fminf(gz, 100.f));
    e = max(-120, min(120, e));
    ew = e;
    const float S = __uint_as_float((unsigned)(127 - e) << 23);
    cf G0; G0.r = CB.r * S; G0.i = CB.i * S;
    const cf E1 = cexp_p2(zr2, zf, 1.f), E2 = csq(E1), E4 = cexp_p2(zr2, zf, 4.f), E8 = csq(E4);
    const cf E16 = cexp_p2(zr2, zf, 16.f), E32 = csq(E16), E64 = cexp_p2(zr2, zf, 64.f), E128 = csq(E64);
    const cf E256 = cexp_p2(zr2, zf, 256.f), E512 = csq(E256), E1024 = cexp_p2(zr2, zf, 1024.f), E2048 = csq(E1024);

    publish_flag();

    const int sw = (lane >> 1) & 3;
    LAS char* tb = tab + lane * 64;
    auto put = [&](int tile, int half, const cf* v, float isign) {
      u32x4 re, im;
#pragma unroll
      for (int j = 0; j < 4; ++j) { re[j] = pkh(v[2 * j].r, v[2 * j + 1].r); im[j] = pkh(isign * v[2 * j].i, isign * v[2 * j + 1].i); }
      *(LAS u32x4*)(tb + tile * 4096 + ((half ^ sw) * 16)) = re;
      *(LAS u32x4*)(tb + tile * 4096 + (((2 + half) ^ sw) * 16)) = im;
    };
    cf Er[8];
    Er[0].r = 1.f; Er[0].i = 0.f; Er[1] = E1; Er[2] = E2; Er[3] = cmul(E1, E2); Er[4] = E4; Er[5] = cmul(E4, E1); Er[6] = cmul(E4, E2); Er[7] = cmul(E4, Er[3]);
    {
      cf Y[8];
      Y[0].r = 1.f; Y[0].i = 0.f; Y[1] = E8; Y[2] = E16; Y[3] = cmul(E8, E16); Y[4] = E32; Y[5] = cmul(E32, E8); Y[6] = cmul(E32, E16); Y[7] = cmul(E32, Y[3]);
      put(3, 0, Y, 1.f);
      cf Y2[8];
      Y2[0] = E64;
#pragma unroll
      for (int j = 1; j < 8; ++j) Y2[j] = cmul(Y[j], E64);
      put(3, 1, Y2, 1.f);
    }
    cf G[8];
    G[0] = G0; G[1] = cmul(G0, E128); G[2] = cmul(G0, E256); G[3] = cmul(G[1], E256);
#pragma unroll
    for (int j = 0; j < 4; ++j) G[4 + j] = cmul(G[j], E512);
    {
      cf T[8];
      T[0] = G[0];
#pragma unroll
      for (int j = 1; j < 8; ++j) T[j] = cmul(G[0], Er[j]);
      put(0, 0, T, -1.f);
      T[0] = G[1];
#pragma unroll
      for (int j = 1; j < 8; ++j) T[j] = cmul(G[1], Er[j]);
      put(0, 1, T, -1.f);
    }
    {
      cf T[8];
      T[0] = G[2]; T[1] = cmul(G[2], E2); T[2] = cmul(G[2], E4); T[3] = cmul(G[2], Er[6]);
      T[4] = G[3]; T[5] = cmul(G[3], E2); T[6] = cmul(G[3], E4); T[7] = cmul(G[3], Er[6]);
      put(1, 0, T, -1.f);
#pragma unroll
      for (int j = 0; j < 4; ++j) { T[2 * j] = G[4 + j]; T[2 * j + 1] = cmul(G[4 + j], E4); }
      put(1, 1, T, -1.f);
    }
    {
      cf T[8];
#pragma unroll
      for (int j = 0; j < 8; ++j) T[j] = cmul(G[j], E1024);
      put(2, 0, T, -1.f);
    }
    k2048 = wave_sum(G0.r * E2048.r - G0.i * E2048.i);
  }
  __builtin_amdgcn_s_waitcnt(0xC07F);
  __builtin_amdgcn_wave_barrier();
  {
    const int g = lane >> 4, q = (lane & 15) >> 2, p = lane & 3;
    f32x4 acc0 = {0.f, 0.f, 0.f, 0.f}, acc1 = acc0, acc2 = acc0;
    const int part = q & 1, cl = part * 2 + (p >> 1);
#pragma unroll
    for (int s = 0; s < 4; ++s) {
      const int m0 = 16 * s + 8 * (g >> 1) + 2 * (g & 1) + (q >> 1), m1 = m0 + 4;
      const int o0 = m0 * 64 + ((cl ^ ((m0 >> 1) & 3)) * 16) + (p & 1) * 8;
      const int o1 = m1 * 64 + ((cl ^ ((m1 >> 1) & 3)) * 16) + (p & 1) * 8;
      u32x4 bb = tr_read2(tab + 3 * 4096 + o0, tab + 3 * 4096 + o1);
      u32x4 a0 = tr_read2(tab + o0, tab + o1);
      u32x4 a1 = tr_read2(tab + 4096 + o0, tab + 4096 + o1);
      u32x4 a2 = tr_read2(tab + 8192 + o0, tab + 8192 + o1);
      acc0 = mfma16h(a0, bb, acc0); acc1 = mfma16h(a1, bb, acc1); acc2 = mfma16h(a2, bb, acc2);
    }
    __builtin_amdgcn_s_waitcnt(0xC07F);
    __builtin_amdgcn_wave_barrier();
    *(LAS f32x4*)(tab + lane * 16) = acc0;
    *(LAS f32x4*)(tab + 1024 + lane * 16) = acc1;
    *(LAS f32x4*)(tab + 2048 + lane * 16) = acc2;
    if (lane == 0) { *(LAS int*)(smem + A_SC + w * 8) = ew; *(LAS float*)(smem + A_SC + w * 8 + 4) = k2048; }
  }
  __syncthreads();
  const int t = tid & 255;
  float SKinv;
  {
    int e4[4]; float k4[4];
#pragma unroll
    for (int j = 0; j < 4; ++j) { e4[j] = *(LAS int*)(smem + A_SC + (c2 * 4 + j) * 8); k4[j] = *(LAS float*)(smem + A_SC + (c2 * 4 + j) * 8 + 4); }
    const int em = max(max(e4[0], e4[1]), max(e4[2], e4[3]));
    float fw[4];
#pragma unroll
    for (int j = 0; j < 4; ++j) { int d = max(e4[j] - em + 5, -120); fw[j] = __uint_as_float((unsigned)(127 + d) << 23); }
    SKinv = __uint_as_float((unsigned)(127 + em - 5) << 23);
    LAS char* kdc = smem + A_KDC + c2 * 1312;
    if (t < 192) {
      const int tile = t >> 6, ln = t & 63, c = ln & 15, g = ln >> 4;
      f32x4 s = {0.f, 0.f, 0.f, 0.f};
#pragma unroll
      for (int j = 0; j < 4; ++j) { f32x4 v = *(LAS f32x4*)(smem + A_TAB + (c2 * 4 + j) * 16384 + tile * 1024 + ln * 16); s += v * fw[j]; }
      const unsigned p01 = pkh(s[0], s[1]), p23 = pkh(s[2], s[3]);
      if (tile == 0) {
        u32x2 v = {p01, p23}; *(LAS u32x2*)(kdc + (128 * (g >> 1) + 8 * c + 4 * (g & 1)) * 2) = v;
      } else if (tile == 1) {
        if (g < 2) { u32x2 v = {p01, p23}; *(LAS u32x2*)(kdc + (256 + 64 * g + 4 * c) * 2) = v; }
        else { const int i0 = 384 + 64 * (g - 2) + 2 * c; *(LAS unsigned*)(kdc + i0 * 2) = p01; *(LAS unsigned*)(kdc + (i0 + 32) * 2) = p23; }
      } else if (g < 2) {
        const int i0 = 512 + 64 * g + c;
        *(LAS unsigned short*)(kdc + i0 * 2) = (unsigned short)p01; *(LAS unsigned short*)(kdc + (i0 + 16) * 2) = (unsigned short)(p01 >> 16);
        *(LAS unsigned short*)(kdc + (i0 + 32) * 2) = (unsigned short)p23; *(LAS unsigned short*)(kdc + (i0 + 48) * 2) = (unsigned short)(p23 >> 16);
      }
    } else if (t == 192) {
      const float k = k4[0] * fw[0] + k4[1] * fw[1] + k4[2] * fw[2] + k4[3] * fw[3];
      *(LAS unsigned*)(kdc + 640 * 2) = pkh(k, 0.f);
    }
  }
  if (w == 7) {
    auto rf = __builtin_amdgcn_make_buffer_rsrc((void*)(ws + WS_FLAGA), 0, 2048, 0x00020000);
    const unsigned tl = (unsigned)tag, th = (unsigned)(tag >> 32);
    for (unsigned spins = 0;; ++spins) {
      u32x4 f0 = __builtin_amdgcn_raw_buffer_load_b128(rf, lane * 32, 0, 16);
      u32x4 f1 = __builtin_amdgcn_raw_buffer_load_b128(rf, lane * 32 + 16, 0, 16);
      const bool ok = f0[0] == tl && f0[1] == th && f0[2] == tl && f0[3] == th && f1[0] == tl && f1[1] == th && f1[2] == tl && f1[3] == th;
      if (__all(ok)) break;
      if (spins > kSpinCap) { if (lane == 0) __hip_atomic_store((GAS unsigned*)(ws + WS_TMO), 1u, __ATOMIC_RELAXED, __HIP_MEMORY_SCOPE_AGENT); break; }
      __builtin_amdgcn_s_sleep(2);
    }
    if (b == 0 && lane == 0) __hip_atomic_store((GAS u64*)(ws + WS_EPOCH), tag, __ATOMIC_RELAXED, __HIP_MEMORY_SCOPE_AGENT);
  }
  __syncthreads();
  u32x4 uu, xx;
  {
    auto rs = __builtin_amdgcn_make_buffer_rsrc((void*)(ws + WS_UX), 0, D * 256 * 32, 0x00020000);
    uu = __builtin_amdgcn_raw_buffer_load_b128(rs, (ch * 256 + t) * 32, 0, 16);
    xx = __builtin_amdgcn_raw_buffer_load_b128(rs, (ch * 256 + t) * 32 + 16, 0, 16);
  }
  {
    const LAS char* kdc = smem + A_KDC + c2 * 1312;
    LAS char* toe = smem + A_TOE2 + c2 * TOE_CH;
    for (int c = t; c < 259; c += 256) {
      unsigned v[11];
#pragma unroll
      for (int e = 0; e < 11; ++e) {
        const int lag = 2048 - 8 * c + 3 - e;
        const unsigned bits = pkbf((float)lag, 0.f) & 0xffffu;
        int idx = lag < 256 ? lag : (int)(bits - (0x4300u - 128u));
        if (lag < 0 || lag > 2047) idx = 641;
        v[e] = *(const LAS unsigned short*)(kdc + idx * 2);
      }
      unsigned ev[5], od[5];
#pragma unroll
      for (int j = 0; j < 5; ++j) { ev[j] = v[2 * j] | (v[2 * j + 1] << 16); od[j] = v[2 * j + 1] | (v[2 * j + 2] << 16); }
      u32x4 c3 = {ev[0], ev[1], ev[2], ev[3]}, c1 = {ev[1], ev[2], ev[3], ev[4]}, c2v = {od[0], od[1], od[2], od[3]}, c0 = {od[1], od[2], od[3], od[4]};
      *(LAS u32x4*)(toe + 0 * TOE_CP + c * 16) = c0;
      *(LAS u32x4*)(toe + 1 * TOE_CP + c * 16) = c1;
      *(LAS u32x4*)(toe + 2 * TOE_CP + c * 16) = c2v;
      *(LAS u32x4*)(toe + 3 * TOE_CP + c * 16) = c3;
    }
    LAS char* u16 = smem + A_U16 + c2 * 4608;
    if (t < 32) { u32x4 z = {0u, 0u, 0u, 0u}; *(LAS u32x4*)(u16 + t * 16) = z; }
    u32x4 uh;
#pragma unroll
    for (int j = 0; j < 4; ++j) uh[j] = pkh(bflo(uu[j]), bfhi(uu[j]));
    *(LAS u32x4*)(u16 + 512 + t * 16) = uh;
  }
  __syncthreads();
  {
    const int bq = lane & 15, g = lane >> 4;
    f32x4 acc[8];
#pragma unroll
    for (int i = 0; i < 8; ++i) acc[i] = f32x4{0.f, 0.f, 0.f, 0.f};
    const LAS char* toe = smem + A_TOE2 + c2 * TOE_CH + (bq & 3) * TOE_CP + (2032 - 4 * (bq >> 2) + 8 * g) * 2;
    const LAS char* ub = smem + A_U16 + c2 * 4608 + (256 + 16 * (bq - 1) + 8 * g) * 2;
#pragma unroll
    for (int it = 0; it < 2; ++it) {
      const int m0 = w4 + 4 * it;
      const LAS char* ta = toe - 64 * m0 - 512 * 7;
      const LAS char* ua = ub - 64 * m0;
      u32x4 A[8], B[8];
#pragma unroll
      for (int k = 0; k < 8; ++k) {
        u32x2 lo = *(const LAS u32x2*)(ta + (7 - k) * 512), hi = *(const LAS u32x2*)(ta + (7 - k) * 512 + 8);
        A[k] = u32x4{lo[0], lo[1], hi[0], hi[1]};
        B[k] = *(const LAS u32x4*)(ua + k * 512);
      }
#pragma unroll
      for (int k = 0; k < 8; ++k)
#pragma unroll
        for (int i = k; i < 8; ++i) acc[i] = mfma16h(A[k], B[i - k], acc[i]);
    }
    LAS char* yp = smem + A_YP + w * 8192;
#pragma unroll
    for (int i = 0; i < 8; ++i) *(LAS f32x4*)(yp + i * 1024 + bq * 64 + g * 16) = acc[i];
  }
  __syncthreads();
  {
    float y[8];
#pragma unroll
    for (int j = 0; j < 8; ++j) y[j] = 0.f;
#pragma unroll
    for (int j = 0; j < 4; ++j) {
      const LAS char* yp = smem + A_YP + (c2 * 4 + j) * 8192 + t * 32;
      f32x4 a = *(const LAS f32x4*)yp, c = *(const LAS f32x4*)(yp + 16);
#pragma unroll
      for (int e = 0; e < 4; ++e) { y[e] += a[e]; y[4 + e] += c[e]; }
    }
    u32x4 hv;
#pragma unroll
    for (int j = 0; j < 4; ++j) {
      const unsigned yb = pkbf(y[2 * j] * SKinv, y[2 * j + 1] * SKinv);
      const unsigned sk = pkbf(bflo(uu[j]) * dsk, bfhi(uu[j]) * dsk);
      const unsigned y2 = pkbf(bflo(yb) + bflo(sk), bfhi(yb) + bfhi(sk));
      hv[j] = pkbf(bflo(xx[j]) + bflo(y2), bfhi(xx[j]) + bfhi(y2));
    }
    *(u32x4*)(ws + WS_HT + ((size_t)ch * L + 8 * t) * 2) = hv;
  }
}

constexpr int G1_HT = 0;
constexpr int G1_ST = 57344;
constexpr int G1_CS = G1_ST + 2048;
constexpr int G1_LDS = G1_CS + 1024;

__global__ __launch_bounds__(256, 1) void k_g1(char* __restrict__ ws) {
  __shared__ __attribute__((aligned(16))) char smem_[G1_LDS];
  LAS char* smem = (LAS char*)smem_;
  const int tid = threadIdx.x, lane = tid & 63;
  const int w = __builtin_amdgcn_readfirstlane(tid >> 6);
  const int xcd = blockIdx.x & 7, idx = blockIdx.x >> 3;
  const int rt = xcd * 4 + idx / 7, j = idx % 7;
  u32x4 hv[14];
  const char* ht = ws + WS_HT + (size_t)(tid >> 3) * (L * 2) + rt * 128 + (tid & 7) * 16;
#pragma unroll
  for (int p = 0; p < 14; ++p) hv[p] = *(const u32x4*)(ht + (size_t)p * 32 * (L * 2));
  const int ft0 = j * 8 + w * 2;
  const char* wf = ws + WS_W1F + (size_t)ft0 * 28 * 1024 + lane * 16;
  u32x4 wa[2][28];
#pragma unroll
  for (int ks = 0; ks < 28; ++ks) { wa[0][ks] = *(const u32x4*)(wf + ks * 1024); wa[1][ks] = *(const u32x4*)(wf + (28 + ks) * 1024); }
  {
    const int r = tid >> 3, c = tid & 7;
#pragma unroll
    for (int p = 0; p < 14; ++p) { const int row = 32 * p + r; *(LAS u32x4*)(smem + G1_HT + row * 128 + ((c ^ (4 * ((row >> 1) & 1))) * 16)) = hv[p]; }
  }
  __syncthreads();
  const int hh = lane >> 5, blk = (lane >> 4) & 1, q = (lane & 15) >> 2, p = lane & 3;
  const int sw = (q >> 1) & 1;
  const LAS char* b0 = smem + G1_HT + (8 * hh + q) * 128 + ((2 * blk + (p >> 1) + 4 * sw) * 16) + (p & 1) * 8;
  const LAS char* b1 = smem + G1_HT + (8 * hh + q) * 128 + ((2 * blk + (p >> 1) + 4 * (1 - sw)) * 16) + (p & 1) * 8;
  f32x16 acc[2][2];
#pragma unroll
  for (int a = 0; a < 2; ++a)
#pragma unroll
    for (int c = 0; c < 2; ++c)
#pragma unroll
      for (int e = 0; e < 16; ++e) acc[a][c][e] = 0.f;
  float s0 = 0.f, s1 = 0.f, q0 = 0.f, q1 = 0.f, cs0 = 0.f, cs1 = 0.f;
  const unsigned ONES = 0x3f803f80u;
#pragma unroll
  for (int ks = 0; ks < 28; ++ks) {
    u32x4 f0 = tr_read2(b0 + ks * 2048, b0 + ks * 2048 + 512);
    u32x4 f1 = tr_read2(b1 + ks * 2048, b1 + ks * 2048 + 512);
    acc[0][0] = mfma32b(wa[0][ks], f0, acc[0][0]);
    acc[0][1] = mfma32b(wa[0][ks], f1, acc[0][1]);
    acc[1][0] = mfma32b(wa[1][ks], f0, acc[1][0]);
    acc[1][1] = mfma32b(wa[1][ks], f1, acc[1][1]);
#pragma unroll
    for (int e = 0; e < 4; ++e) { cs0 = dot2bf(wa[0][ks][e], ONES, cs0); cs1 = dot2bf(wa[1][ks][e], ONES, cs1); }
    if ((ks & 3) == w) {
#pragma unroll
      for (int e = 0; e < 4; ++e) { s0 = dot2bf(f0[e], ONES, s0); q0 = dot2bf(f0[e], f0[e], q0); s1 = dot2bf(f1[e], ONES, s1); q1 = dot2bf(f1[e], f1[e], q1); }
    }
  }
  s0 += __shfl_xor(s0, 32); s1 += __shfl_xor(s1, 32); q0 += __shfl_xor(q0, 32); q1 += __shfl_xor(q1, 32);
  cs0 += __shfl_xor(cs0, 32); cs1 += __shfl_xor(cs1, 32);
  if (hh == 0) {
    *(LAS u32x2*)(smem + G1_ST + (w * 64 + lane) * 8) = u32x2{__float_as_uint(s0), __float_as_uint(q0)};
    *(LAS u32x2*)(smem + G1_ST + (w * 64 + 32 + lane) * 8) = u32x2{__float_as_uint(s1), __float_as_uint(q1)};
    *(LAS float*)(smem + G1_CS + (w * 64 + lane) * 4) = cs0;
    *(LAS float*)(smem + G1_CS + (w * 64 + 32 + lane) * 4) = cs1;
  }
  __syncthreads();
  float mb[2], ri[2];
#pragma unroll
  for (int c = 0; c < 2; ++c) {
    float s = 0.f, qq = 0.f;
#pragma unroll
    for (int ww = 0; ww < 4; ++ww) { u32x2 v = *(const LAS u32x2*)(smem + G1_ST + (ww * 64 + c * 32 + (lane & 31)) * 8); s += __uint_as_float(v[0]); qq += __uint_as_float(v[1]); }
    const float mean = s * (1.0f / D);
    const float var = fmaxf(qq * (1.0f / D) - mean * mean, 0.f);
    ln_scalars(mean, var, mb[c], ri[c]);
  }
  char* h1 = ws + WS_H1F + (size_t)rt * 2 * 112 * 1024 + lane * 16;
#pragma unroll
  for (int a = 0; a < 2; ++a) {
    float cs[16];
#pragma unroll
    for (int g4 = 0; g4 < 4; ++g4) {
      f32x4 v = *(const LAS f32x4*)(smem + G1_CS + (w * 64 + a * 32 + 8 * g4 + 4 * hh) * 4);
#pragma unroll
      for (int e = 0; e < 4; ++e) cs[4 * g4 + e] = v[e];
    }
#pragma unroll
    for (int c = 0; c < 2; ++c) {
#pragma unroll
      for (int s = 0; s < 2; ++s) {
        u32x4 fr;
#pragma unroll
        for (int e = 0; e < 4; ++e) {
          const int r0 = 8 * s + 2 * e;
          const float v0 = (acc[a][c][r0] - mb[c] * cs[r0]) * ri[c], v1 = (acc[a][c][r0 + 1] - mb[c] * cs[r0 + 1]) * ri[c];
          fr[e] = pkbf(fmaxf(v0, 0.f), fmaxf(v1, 0.f));
        }
        const int fs = 2 * (ft0 + a) + s;
        *(u32x4*)(h1 + ((size_t)c * 112 + fs) * 1024) = fr;
      }
    }
  }
}

constexpr int G2_P = 0;
constexpr int G2_H = 4 * 64 * 272;
constexpr int G2_LDS = G2_H + 8192;

__global__ __launch_bounds__(256, 1) void k_g2(char* __restrict__ ws, unsigned short* __restrict__ out) {
  __shared__ __attribute__((aligned(16))) char smem_[G2_LDS];
  LAS char* smem = (LAS char*)smem_;
  const int tid = threadIdx.x, lane = tid & 63;
  const int w = __builtin_amdgcn_readfirstlane(tid >> 6);
  const int xcd = blockIdx.x & 7, idx = blockIdx.x >> 3;
  const int rt = xcd * 4 + idx / 7, j = idx % 7;
  const char* hp = ws + WS_H1F + ((size_t)rt * 2 * 112 + 28 * w) * 1024 + lane * 16;
  const char* wp = ws + WS_W2F + ((size_t)(2 * j) * 112 + 28 * w) * 1024 + lane * 16;
  {
#pragma unroll
    for (int p = 0; p < 2; ++p) {
      const int c = tid + 256 * p, row = c >> 3, cc = c & 7;
      u32x4 v = *(const u32x4*)(ws + WS_HT + (size_t)(64 * j + row) * (L * 2) + rt * 128 + cc * 16);
      *(LAS u32x4*)(smem + G2_H + row * 128 + cc * 16) = v;
    }
  }
  f32x16 acc[2][2];
#pragma unroll
  for (int a = 0; a < 2; ++a)
#pragma unroll
    for (int c = 0; c < 2; ++c)
#pragma unroll
      for (int e = 0; e < 16; ++e) acc[a][c][e] = 0.f;
#pragma unroll
  for (int ks = 0; ks < 28; ++ks) {
    u32x4 a0 = *(const u32x4*)(wp + ks * 1024), a1 = *(const u32x4*)(wp + (112 + ks) * 1024);
    u32x4 f0 = *(const u32x4*)(hp + ks * 1024), f1 = *(const u32x4*)(hp + (112 + ks) * 1024);
    acc[0][0] = mfma32b(a0, f0, acc[0][0]);
    acc[0][1] = mfma32b(a0, f1, acc[0][1]);
    acc[1][0] = mfma32b(a1, f0, acc[1][0]);
    acc[1][1] = mfma32b(a1, f1, acc[1][1]);
  }
  const int hh = lane >> 5;
#pragma unroll
  for (int a = 0; a < 2; ++a)
#pragma unroll
    for (int c = 0; c < 2; ++c)
#pragma unroll
      for (int g4 = 0; g4 < 4; ++g4) {
        f32x4 v = {acc[a][c][4 * g4], acc[a][c][4 * g4 + 1], acc[a][c][4 * g4 + 2], acc[a][c][4 * g4 + 3]};
        *(LAS f32x4*)(smem + G2_P + (w * 64 + c * 32 + (lane & 31)) * 272 + (a * 32 + 8 * g4 + 4 * hh) * 4) = v;
      }
  __syncthreads();
  {
    const int l = lane, c4 = w;
    float o[16];
#pragma unroll
    for (int e = 0; e < 16; ++e) o[e] = 0.f;
#pragma unroll
    for (int ww = 0; ww < 4; ++ww)
#pragma unroll
      for (int i = 0; i < 4; ++i) {
        f32x4 v = *(const LAS f32x4*)(smem + G2_P + (ww * 64 + l) * 272 + (16 * c4 + 4 * i) * 4);
#pragma unroll
        for (int e = 0; e < 4; ++e) o[4 * i + e] += v[e];
      }
    unsigned pk[8];
#pragma unroll
    for (int e = 0; e < 8; ++e) {
      const float h0 = bf2f(*(const LAS unsigned short*)(smem + G2_H + (16 * c4 + 2 * e) * 128 + l * 2));
      const float h1 = bf2f(*(const LAS unsigned short*)(smem + G2_H + (16 * c4 + 2 * e + 1) * 128 + l * 2));
      const unsigned ob = pkbf(o[2 * e], o[2 * e + 1]);
      pk[e] = pkbf(h0 + bflo(ob), h1 + bfhi(ob));
    }
    char* op = (char*)out + ((size_t)(rt * 64 + l) * D + 64 * j + 16 * c4) * 2;
    *(u32x4*)op = u32x4{pk[0], pk[1], pk[2], pk[3]};
    *(u32x4*)(op + 16) = u32x4{pk[4], pk[5], pk[6], pk[7]};
  }
}

}

extern "C" void kernel_launch(void* const* d_in, const int* in_sizes, int n_in, void* d_out, int out_size, void* d_ws, size_t ws_size, hipStream_t stream) {
  typedef const unsigned short* cp;
  k_ssm<<<256, 512, 0, stream>>>((cp)d_in[0], (cp)d_in[1], (cp)d_in[2], (cp)d_in[3], (cp)d_in[4], (cp)d_in[5], (cp)d_in[6], (cp)d_in[7], (cp)d_in[8], (cp)d_in[10], (cp)d_in[11], (char*)d_ws);
  k_g1<<<224, 256, 0, stream>>>((char*)d_ws);
  k_g2<<<224, 256, 0, stream>>>((char*)d_ws, (unsigned short*)d_out);
}
```

```cpp
#include <hip/hip_runtime.h>
#include <stdint.h>

namespace {

typedef __attribute__((ext_vector_type(8))) _Float16 f16x8;
typedef __attribute__((ext_vector_type(4))) _Float16 f16x4;
typedef __attribute__((ext_vector_type(2))) _Float16 f16x2;
typedef __attribute__((ext_vector_type(8))) __bf16 bf16x8;
typedef __attribute__((ext_vector_type(2))) __bf16 bf16x2;
typedef __attribute__((ext_vector_type(4))) float f32x4;
typedef __attribute__((ext_vector_type(16))) float f32x16;
typedef __attribute__((ext_vector_type(4))) unsigned u32x4;
typedef __attribute__((ext_vector_type(2))) unsigned u32x2;
typedef __attribute__((ext_vector_type(4))) short s16x4;
typedef unsigned long long u64;
#define LAS __attribute__((address_space(3)))
#define GAS __attribute__((address_space(1)))
#define DI __device__ __forceinline__

constexpr int L = 2048, D = 448, NM = 256, F = 1792;

constexpr size_t WS_EPOCH = 0;
constexpr size_t WS_TMO = 64;
constexpr size_t WS_FLAGA = 4096;
constexpr size_t WS_FLAGB = 8192;
constexpr size_t WS_UX = 16384;
constexpr size_t WS_HT = WS_UX + (size_t)D * 256 * 32;
constexpr size_t WS_W1F = WS_HT + (size_t)D * L * 2;
constexpr size_t WS_W2F = WS_W1F + (size_t)D * F * 2;
constexpr size_t WS_H1F = WS_W2F + (size_t)D * F * 2;
constexpr size_t WS_END = WS_H1F + (size_t)L * F * 2;
constexpr size_t WS_STAMP = WS_END;
constexpr unsigned kSpinCap = 1u << 20;

#define STAMP(i) do {} while (0)
#define STAMP_DECL
#define STAMP_FLUSH(k) do {} while (0)
#define SB() __builtin_amdgcn_sched_barrier(0)
DI float bf2f(unsigned b) { return __uint_as_float(b << 16); }
DI float bflo(unsigned p) { return __uint_as_float(p << 16); }
DI float bfhi(unsigned p) { return __uint_as_float(p & 0xffff0000u); }
DI unsigned pkbf(float a, float b) { bf16x2 r; r[0] = (__bf16)a; r[1] = (__bf16)b; return __builtin_bit_cast(unsigned, r); }
DI float rbf(float a) { return bflo(pkbf(a, a)); }
DI unsigned pkh(float a, float b) { f16x2 r; r[0] = (_Float16)a; r[1] = (_Float16)b; return __builtin_bit_cast(unsigned, r); }

template <int CTRL> DI float dppf(float v) { return __int_as_float(__builtin_amdgcn_update_dpp(0, __float_as_int(v), CTRL, 0xF, 0xF, true)); }
DI float rdl(float v, int l) { return __int_as_float(__builtin_amdgcn_readlane(__float_as_int(v), l)); }
DI float wave_sum(float v) {
  v += dppf<0xB1>(v); v += dppf<0x4E>(v); v += dppf<0x141>(v); v += dppf<0x140>(v);
  return (rdl(v, 0) + rdl(v, 16)) + (rdl(v, 32) + rdl(v, 48));
}
DI float wave_max(float v) {
  v = fmaxf(v, dppf<0xB1>(v)); v = fmaxf(v, dppf<0x4E>(v)); v = fmaxf(v, dppf<0x141>(v)); v = fmaxf(v, dppf<0x140>(v));
  return fmaxf(fmaxf(rdl(v, 0), rdl(v, 16)), fmaxf(rdl(v, 32), rdl(v, 48)));
}
DI s16x4 tr_read(const LAS char* p) { return __builtin_amdgcn_ds_read_tr16_b64_v4i16((LAS s16x4*)p); }
DI u32x4 tr_read2(const LAS char* p0, const LAS char* p1) {
  s16x4 a = tr_read(p0), b = tr_read(p1);
  u32x2 a2 = __builtin_bit_cast(u32x2, a), b2 = __builtin_bit_cast(u32x2, b);
  u32x4 r; r[0] = a2[0]; r[1] = a2[1]; r[2] = b2[0]; r[3] = b2[1]; return r;
}
DI f32x4 mfma16h(u32x4 a, u32x4 b, f32x4 c) { return __builtin_amdgcn_mfma_f32_16x16x32_f16(__builtin_bit_cast(f16x8, a), __builtin_bit_cast(f16x8, b), c, 0, 0, 0); }
DI f32x16 mfma32b(u32x4 a, u32x4 b, f32x16 c) { return __builtin_amdgcn_mfma_f32_32x32x16_bf16(__builtin_bit_cast(bf16x8, a), __builtin_bit_cast(bf16x8, b), c, 0, 0, 0); }
DI float dot2bf(unsigned a, unsigned b, float c) { return __builtin_amdgcn_fdot2_f32_bf16(__builtin_bit_cast(bf16x2, a), __builtin_bit_cast(bf16x2, b), c, false); }

DI u64 load_epoch(const char* ws) {
  const u32x2 v = *(const u32x2*)(ws + WS_EPOCH);
  unsigned lo = __builtin_amdgcn_readfirstlane(v[0]), hi = __builtin_amdgcn_readfirstlane(v[1]);
  asm volatile("" : "+s"(lo), "+s"(hi));
  return ((u64)hi << 32) | lo;
}
struct cf { float r, i; };
DI cf cmul(cf a, cf b) { cf o; o.r = a.r * b.r - a.i * b.i; o.i = a.r * b.i + a.i * b.r; return o; }
DI cf csq(cf a) { cf o; o.r = a.r * a.r - a.i * a.i; o.i = 2.f * a.r * a.i; return o; }
DI cf cexp_p2(float zr2, float zf, float p2) {
  float m = __builtin_amdgcn_exp2f(zr2 * p2);
  float t = __builtin_amdgcn_fractf(zf * p2);
  cf o; o.r = m * __builtin_amdgcn_cosf(t); o.i = m * __builtin_amdgcn_sinf(t); return o;
}

DI void ln_scalars(float mean, float var, float& mb, float& rinv) {
  mb = rbf(mean);
  float vb = rbf(var);
  float sb = rbf(__builtin_sqrtf(rbf(vb + 1e-5f)));
  rinv = 1.0f / sb;
}

constexpr int A_T = 0;
constexpr int A_TAB = 14336;
constexpr int A_MISC = A_TAB + 8 * 16384;
constexpr int A_KDC = A_MISC;
constexpr int A_SC = A_KDC + 2 * 1312;
constexpr int A_LDS = A_SC + 128;
constexpr int TOE_CP = 4160, TOE_CH = 4 * TOE_CP;
constexpr int A_U16 = A_TAB;
constexpr int A_TOE2 = A_TAB + 9216;
constexpr int A_YP = A_TAB + 42496;
constexpr int A_RT = A_TAB + 108032;

__global__ __launch_bounds__(512, 2) void k_ssm(const unsigned short* __restrict__ x, const unsigned short* __restrict__ Are, const unsigned short* __restrict__ Aim,
                                                 const unsigned short* __restrict__ Bre, const unsigned short* __restrict__ Bim, const unsigned short* __restrict__ Cre,
                                                 const unsigned short* __restrict__ Cim, const unsigned short* __restrict__ logdt, const unsigned short* __restrict__ Dsk,
                                                 const unsigned short* __restrict__ w1, const unsigned short* __restrict__ w2, char* __restrict__ ws) {
  __shared__ __attribute__((aligned(16))) char smem_[A_LDS];
  LAS char* smem = (LAS char*)smem_;
  const int tid = threadIdx.x, lane = tid & 63;
  const int w = __builtin_amdgcn_readfirstlane(tid >> 6);
  const int b = blockIdx.x;
  const bool has_ch = b < 224;
  const int c2 = w >> 2, w4 = w & 3;
  const int ch = 2 * b + c2;
  STAMP_DECL
  STAMP(0);
  const int row = 8 * b + w;
  u32x4 xv = *(const u32x4*)(x + (size_t)row * D + min(lane, 55) * 8);
  const int chs = has_ch ? ch : 0;
  const int po = chs * NM + w4 * 64 + lane;
  const unsigned short r_are = Are[po], r_aim = Aim[po], r_bre = Bre[po], r_bim = Bim[po], r_cre = Cre[po], r_cim = Cim[po];
  const unsigned short r_ldt = logdt[chs], r_dsk = Dsk[chs];
  const int gw = b * 8 + w;
  const bool has_rt = gw < 1568;
  const int gwc = has_rt ? gw : 0;
  const bool is_w1 = gwc < 784;
  const int g2 = gwc - 784;
  const int rt_ks = is_w1 ? gwc / 28 : g2 / 7, rt_nb = is_w1 ? gwc % 28 : g2 % 7;
  const int rld = is_w1 ? F : D;
  const unsigned short* rsrc_ = (is_w1 ? w1 : w2) + (size_t)(16 * rt_ks) * rld + 64 * rt_nb + (size_t)(lane >> 3) * rld + (lane & 7) * 8;
  const u32x4 rt0 = *(const u32x4*)rsrc_;
  const u32x4 rt1 = *(const u32x4*)(rsrc_ + (size_t)8 * rld);
  const u32x2 ev = *(const u32x2*)(ws + WS_EPOCH);
  SB();
  if (lane >= 56) xv = u32x4{0u, 0u, 0u, 0u};
  if (tid == 0) *(LAS unsigned*)(smem + A_SC + 64) = 0u;

  {
    float xf[8];
#pragma unroll
    for (int j = 0; j < 4; ++j) { xf[2 * j] = bflo(xv[j]); xf[2 * j + 1] = bfhi(xv[j]); }
    float s = 0.f;
#pragma unroll
    for (int j = 0; j < 8; ++j) s += xf[j];
    const float mean = wave_sum(s) * (1.0f / D);
    float q = 0.f;
#pragma unroll
    for (int j = 0; j < 8; ++j) { float d = xf[j] - mean; q += d * d; }
    if (lane >= 56) q = 0.f;
    const float var = wave_sum(q) * (1.0f / D);
    float mb, rinv; ln_scalars(mean, var, mb, rinv);
    u32x4 uv;
#pragma unroll
    for (int j = 0; j < 4; ++j) {
      unsigned t = pkbf(xf[2 * j] - mb, xf[2 * j + 1] - mb);
      uv[j] = pkbf(bflo(t) * rinv, bfhi(t) * rinv);
    }
    if (lane < 56) {
      *(LAS u32x4*)(smem + A_T + w * 896 + lane * 16) = uv;
      *(LAS u32x4*)(smem + A_T + 7168 + w * 896 + lane * 16) = xv;
    }
  }
  STAMP(1);
  __syncthreads();
  if (w < 7) {
    const int g = lane >> 4, q = (lane & 15) >> 2, p = lane & 3;
    const LAS char* a0 = smem + A_T + q * 896 + (64 * w + 16 * g + 4 * p) * 2;
    u32x4 uu = tr_read2(a0, a0 + 4 * 896);
    u32x4 xx = tr_read2(a0 + 7168, a0 + 7168 + 4 * 896);
    const int d = 64 * w + lane;
    auto rs = __builtin_amdgcn_make_buffer_rsrc((void*)(ws + WS_UX), 0, D * 256 * 32, 0x00020000);
    __builtin_amdgcn_raw_buffer_store_b128(uu, rs, (d * 256 + b) * 32, 0, 16);
    __builtin_amdgcn_raw_buffer_store_b128(xx, rs, (d * 256 + b) * 32 + 16, 0, 16);
  }
  STAMP(2);
  unsigned elo = __builtin_amdgcn_readfirstlane(ev[0]), ehi = __builtin_amdgcn_readfirstlane(ev[1]);
  asm volatile("" : "+s"(elo), "+s"(ehi));
  const u64 tag = (((u64)ehi << 32) | elo) + 1;
  auto publish_flag = [&]() {
    asm volatile("s_waitcnt vmcnt(0)" ::: "memory");
    unsigned old = 0;
    if (lane == 0) old = __hip_atomic_fetch_add((LAS unsigned*)(smem + A_SC + 64), 1u, __ATOMIC_RELAXED, __HIP_MEMORY_SCOPE_WORKGROUP);
    old = __builtin_amdgcn_readfirstlane(old);
    if (old == 7u && lane == 0) __hip_atomic_store((GAS u64*)(ws + WS_FLAGA) + b, tag, __ATOMIC_RELAXED, __HIP_MEMORY_SCOPE_AGENT);
  };
  auto retile = [&]() {
    if (has_rt) {
      LAS char* sc = smem + A_RT + w * 2048;
      *(LAS u32x4*)(sc + lane * 16) = rt0;
      *(LAS u32x4*)(sc + 1024 + lane * 16) = rt1;
      __builtin_amdgcn_s_waitcnt(0xC07F);
      __builtin_amdgcn_wave_barrier();
      const int g = lane >> 4, blk = g & 1, hh = g >> 1, q = (lane & 15) >> 2, p = lane & 3;
      const int r0 = is_w1 ? (8 * hh + q) : (4 * hh + q);
      const int r1 = is_w1 ? (8 * hh + 4 + q) : (8 + 4 * hh + q);
#pragma unroll
      for (int t = 0; t < 2; ++t) {
        const int cb = (t * 32 + blk * 16 + 4 * p) * 2;
        u32x4 fr = tr_read2(sc + r0 * 128 + cb, sc + r1 * 128 + cb);
        char* dst = is_w1 ? (ws + WS_W1F + ((size_t)(2 * rt_nb + t) * 28 + rt_ks) * 1024) : (ws + WS_W2F + ((size_t)(2 * rt_nb + t) * 112 + rt_ks) * 1024);
        *(u32x4*)(dst + lane * 16) = fr;
      }
    }
  };
  if (!has_ch) { publish_flag(); retile(); return; }

  LAS char* tab = smem + A_TAB + w * 16384;
  float k2048;
  int ew;
  {
    const float are = bf2f(r_are), aim = bf2f(r_aim), bre = bf2f(r_bre), bim = bf2f(r_bim), cre = bf2f(r_cre), cim = bf2f(r_cim);
    const float dt = rbf(expf(bf2f(r_ldt)));
    const float zr = dt * are, zi = dt * aim;
    const float zr2 = zr * 1.44269504088896341f, zf = zi * 0.15915494309189535f;
    const float dbr = dt * bre, dbi = dt * bim;
    cf CB; CB.r = cre * dbr - cim * dbi; CB.i = cre * dbi + cim * dbr;
    const float mx = wave_max(fmaxf(fabsf(CB.r), fabsf(CB.i)));
    const float gz = fmaxf(wave_max(zr2), 0.f) * 2048.f;
    int e = (mx > 0.f) ? ((int)(__float_as_uint(mx) >> 23) - 127) : 0;
    e += (int)ceilf(fminf(gz, 100.f));
    e = max(-120, min(120, e));
    ew = e;
    const float S = __uint_as_float((unsigned)(127 - e) << 23);
    cf G0; G0.r = CB.r * S; G0.i = CB.i * S;
    const cf E1 = cexp_p2(zr2, zf, 1.f), E2 = csq(E1), E4 = cexp_p2(zr2, zf, 4.f), E8 = csq(E4);
    const cf E16 = cexp_p2(zr2, zf, 16.f), E32 = csq(E16), E64 = cexp_p2(zr2, zf, 64.f), E128 = csq(E64);
    const cf E256 = cexp_p2(zr2, zf, 256.f), E512 = csq(E256), E1024 = cexp_p2(zr2, zf, 1024.f), E2048 = csq(E1024);
    const int sw = (lane >> 1) & 3;
    LAS char* tb = tab + lane * 64;
    auto put = [&](int tile, int half, const cf* v, float isign) {
      u32x4 re, im;
#pragma unroll
      for (int j = 0; j < 4; ++j) { re[j] = pkh(v[2 * j].r, v[2 * j + 1].r); im[j] = pkh(isign * v[2 * j].i, isign * v[2 * j + 1].i); }
      *(LAS u32x4*)(tb + tile * 4096 + ((half ^ sw) * 16)) = re;
      *(LAS u32x4*)(tb + tile * 4096 + (((2 + half) ^ sw) * 16)) = im;
    };
    cf Er[8];
    Er[0].r = 1.f; Er[0].i = 0.f; Er[1] = E1; Er[2] = E2; Er[3] = cmul(E1, E2); Er[4] = E4; Er[5] = cmul(E4, E1); Er[6] = cmul(E4, E2); Er[7] = cmul(E4, Er[3]);
    {
      cf Y[8];
      Y[0].r = 1.f; Y[0].i = 0.f; Y[1] = E8; Y[2] = E16; Y[3] = cmul(E8, E16); Y[4] = E32; Y[5] = cmul(E32, E8); Y[6] = cmul(E32, E16); Y[7] = cmul(E32, Y[3]);
      put(3, 0, Y, 1.f);
      cf Y2[8];
      Y2[0] = E64;
#pragma unroll
      for (int j = 1; j < 8; ++j) Y2[j] = cmul(Y[j], E64);
      put(3, 1, Y2, 1.f);
    }
    SB();
    STAMP(3);
    publish_flag();
    SB();
    cf G[8];
    G[0] = G0; G[1] = cmul(G0, E128); G[2] = cmul(G0, E256); G[3] = cmul(G[1], E256);
#pragma unroll
    for (int j = 0; j < 4; ++j) G[4 + j] = cmul(G[j], E512);
    {
      cf T[8];
      T[0] = G[0];
#pragma unroll
      for (int j = 1; j < 8; ++j) T[j] = cmul(G[0], Er[j]);
      put(0, 0, T, -1.f);
      T[0] = G[1];
#pragma unroll
      for (int j = 1; j < 8; ++j) T[j] = cmul(G[1], Er[j]);
      put(0, 1, T, -1.f);
    }
    {
      cf T[8];
      T[0] = G[2]; T[1] = cmul(G[2], E2); T[2] = cmul(G[2], E4); T[3] = cmul(G[2], Er[6]);
      T[4] = G[3]; T[5] = cmul(G[3], E2); T[6] = cmul(G[3], E4); T[7] = cmul(G[3], Er[6]);
      put(1, 0, T, -1.f);
#pragma unroll
      for (int j = 0; j < 4; ++j) { T[2 * j] = G[4 + j]; T[2 * j + 1] = cmul(G[4 + j], E4); }
      put(1, 1, T, -1.f);
    }
    {
      cf T[8];
#pragma unroll
      for (int j = 0; j < 8; ++j) T[j] = cmul(G[j], E1024);
      put(2, 0, T, -1.f);
    }
    k2048 = wave_sum(G0.r * E2048.r - G0.i * E2048.i);
  }
  STAMP(4);
  __builtin_amdgcn_s_waitcnt(0xC07F);
  __builtin_amdgcn_wave_barrier();
  {
    const int g = lane >> 4, q = (lane & 15) >> 2, p = lane & 3;
    f32x4 acc0 = {0.f, 0.f, 0.f, 0.f}, acc1 = acc0, acc2 = acc0;
    const int part = q & 1, cl = part * 2 + (p >> 1);
    u32x4 bb[4], a0[4], a1[4], a2[4];
#pragma unroll
    for (int s = 0; s < 4; ++s) {
      const int m0 = 16 * s + 8 * (g >> 1) + 2 * (g & 1) + (q >> 1), m1 = m0 + 4;
      const int o0 = m0 * 64 + ((cl ^ ((m0 >> 1) & 3)) * 16) + (p & 1) * 8;
      const int o1 = m1 * 64 + ((cl ^ ((m1 >> 1) & 3)) * 16) + (p & 1) * 8;
      bb[s] = tr_read2(tab + 3 * 4096 + o0, tab + 3 * 4096 + o1);
      a0[s] = tr_read2(tab + o0, tab + o1);
      a1[s] = tr_read2(tab + 4096 + o0, tab + 4096 + o1);
      a2[s] = tr_read2(tab + 8192 + o0, tab + 8192 + o1);
    }
    SB();
#pragma unroll
    for (int s = 0; s < 4; ++s) { acc0 = mfma16h(a0[s], bb[s], acc0); acc1 = mfma16h(a1[s], bb[s], acc1); acc2 = mfma16h(a2[s], bb[s], acc2); }
    SB();
    *(LAS f32x4*)(tab + lane * 16) = acc0;
    *(LAS f32x4*)(tab + 1024 + lane * 16) = acc1;
    *(LAS f32x4*)(tab + 2048 + lane * 16) = acc2;
    if (lane == 0) { *(LAS int*)(smem + A_SC + w * 8) = ew; *(LAS float*)(smem + A_SC + w * 8 + 4) = k2048; }
  }
  STAMP(5);
  STAMP(6);
  __syncthreads();
  STAMP(7);
  const int t = tid & 255;
  u32x4 uu, xx;
  float SKinv;
  {
    int e4[4]; float k4[4];
#pragma unroll
    for (int j = 0; j < 4; ++j) { e4[j] = *(LAS int*)(smem + A_SC + (c2 * 4 + j) * 8); k4[j] = *(LAS float*)(smem + A_SC + (c2 * 4 + j) * 8 + 4); }
    const int em = max(max(e4[0], e4[1]), max(e4[2], e4[3]));
    float fw[4];
#pragma unroll
    for (int j = 0; j < 4; ++j) { int d = max(e4[j] - em + 5, -120); fw[j] = __uint_as_float((unsigned)(127 + d) << 23); }
    SKinv = __uint_as_float((unsigned)(127 + em - 5) << 23);
    LAS char* kdc = smem + A_KDC + c2 * 1312;
    if (t < 192) {
      const int tile = t >> 6, ln = t & 63, c = ln & 15, g = ln >> 4;
      f32x4 s = {0.f, 0.f, 0.f, 0.f};
#pragma unroll
      for (int j = 0; j < 4; ++j) { f32x4 v = *(LAS f32x4*)(smem + A_TAB + (c2 * 4 + j) * 16384 + tile * 1024 + ln * 16); s += v * fw[j]; }
      const unsigned p01 = pkh(s[0], s[1]), p23 = pkh(s[2], s[3]);
      if (tile == 0) {
        u32x2 v = {p01, p23}; *(LAS u32x2*)(kdc + (128 * (g >> 1) + 8 * c + 4 * (g & 1)) * 2) = v;
      } else if (tile == 1) {
        if (g < 2) { u32x2 v = {p01, p23}; *(LAS u32x2*)(kdc + (256 + 64 * g + 4 * c) * 2) = v; }
        else { const int i0 = 384 + 64 * (g - 2) + 2 * c; *(LAS unsigned*)(kdc + i0 * 2) = p01; *(LAS unsigned*)(kdc + (i0 + 32) * 2) = p23; }
      } else if (g < 2) {
        const int i0 = 512 + 64 * g + c;
        *(LAS unsigned short*)(kdc + i0 * 2) = (unsigned short)p01; *(LAS unsigned short*)(kdc + (i0 + 16) * 2) = (unsigned short)(p01 >> 16);
        *(LAS unsigned short*)(kdc + (i0 + 32) * 2) = (unsigned short)p23; *(LAS unsigned short*)(kdc + (i0 + 48) * 2) = (unsigned short)(p23 >> 16);
      }
    } else if (t == 192) {
      const float k = k4[0] * fw[0] + k4[1] * fw[1] + k4[2] * fw[2] + k4[3] * fw[3];
      *(LAS unsigned*)(kdc + 640 * 2) = pkh(k, 0.f);
    }
  }
  STAMP(8);
  __syncthreads();
  STAMP(9);
  {
    const LAS char* kdc = smem + A_KDC + c2 * 1312;
    LAS char* toe = smem + A_TOE2 + c2 * TOE_CH;
    for (int c = t; c < 259; c += 256) {
      unsigned v[11];
#pragma unroll
      for (int e = 0; e < 11; ++e) {
        const int lag = 2048 - 8 * c + 3 - e;
        const unsigned bits = pkbf((float)lag, 0.f) & 0xffffu;
        int idx = lag < 256 ? lag : (int)(bits - (0x4300u - 128u));
        if (lag < 0 || lag > 2047) idx = 641;
        v[e] = *(const LAS unsigned short*)(kdc + idx * 2);
      }
      unsigned ev[5], od[5];
#pragma unroll
      for (int j = 0; j < 5; ++j) { ev[j] = v[2 * j] | (v[2 * j + 1] << 16); od[j] = v[2 * j + 1] | (v[2 * j + 2] << 16); }
      u32x4 c3 = {ev[0], ev[1], ev[2], ev[3]}, c1 = {ev[1], ev[2], ev[3], ev[4]}, c2v = {od[0], od[1], od[2], od[3]}, c0 = {od[1], od[2], od[3], od[4]};
      *(LAS u32x4*)(toe + 0 * TOE_CP + c * 16) = c0;
      *(LAS u32x4*)(toe + 1 * TOE_CP + c * 16) = c1;
      *(LAS u32x4*)(toe + 2 * TOE_CP + c * 16) = c2v;
      *(LAS u32x4*)(toe + 3 * TOE_CP + c * 16) = c3;
    }
    retile();
    STAMP(10);
    {
      const GAS u64* fl = (const GAS u64*)(ws + WS_FLAGA) + t;
      for (unsigned spins = 0;; ++spins) {
        const u64 v = __hip_atomic_load(fl, __ATOMIC_RELAXED, __HIP_MEMORY_SCOPE_AGENT);
        if (__all(v == tag)) break;
        if (spins > kSpinCap) { if (lane == 0) __hip_atomic_store((GAS unsigned*)(ws + WS_TMO), 1u, __ATOMIC_RELAXED, __HIP_MEMORY_SCOPE_AGENT); break; }
        __builtin_amdgcn_s_sleep(1);
      }
      asm volatile("" ::: "memory");
      auto rs = __builtin_amdgcn_make_buffer_rsrc((void*)(ws + WS_UX), 0, D * 256 * 32, 0x00020000);
      uu = __builtin_amdgcn_raw_buffer_load_b128(rs, (ch * 256 + t) * 32, 0, 16);
      xx = __builtin_amdgcn_raw_buffer_load_b128(rs, (ch * 256 + t) * 32 + 16, 0, 16);
    }
    LAS char* u16 = smem + A_U16 + c2 * 4608;
    if (t < 32) { u32x4 z = {0u, 0u, 0u, 0u}; *(LAS u32x4*)(u16 + t * 16) = z; }
    u32x4 uh;
#pragma unroll
    for (int j = 0; j < 4; ++j) uh[j] = pkh(bflo(uu[j]), bfhi(uu[j]));
    *(LAS u32x4*)(u16 + 512 + t * 16) = uh;
  }
  STAMP(11);
  __syncthreads();
  if (b == 0 && tid == 0) __hip_atomic_store((GAS u64*)(ws + WS_EPOCH), tag, __ATOMIC_RELAXED, __HIP_MEMORY_SCOPE_AGENT);
  STAMP(12);
  {
    const int bq = lane & 15, g = lane >> 4;
    f32x4 acc[8];
#pragma unroll
    for (int i = 0; i < 8; ++i) acc[i] = f32x4{0.f, 0.f, 0.f, 0.f};
    const LAS char* tlo = smem + A_TOE2 + c2 * TOE_CH + (bq & 3) * TOE_CP + (2032 - 4 * (bq >> 2) + 8 * g) * 2 - 64 * w4 - 512 * 7 - 256;
    const LAS char* thi = tlo + 8;
    asm volatile("" : "+v"(thi));
    const LAS char* ua = smem + A_U16 + c2 * 4608 + (256 + 16 * (bq - 1) + 8 * g) * 2 - 64 * w4 - 256;
    u32x4 A[2][8], B[2][8];
#pragma unroll
    for (int it = 0; it < 2; ++it) {
#pragma unroll
      for (int k = 0; k < 8; ++k) {
        u32x2 lo = *(const LAS u32x2*)(tlo + (7 - k) * 512 + (1 - it) * 256), hi = *(const LAS u32x2*)(thi + (7 - k) * 512 + (1 - it) * 256);
        A[it][k] = u32x4{lo[0], lo[1], hi[0], hi[1]};
        B[it][k] = *(const LAS u32x4*)(ua + k * 512 + (1 - it) * 256);
      }
    }
    SB();
#pragma unroll
    for (int it = 0; it < 2; ++it) {
#pragma unroll
      for (int k = 0; k < 8; ++k)
#pragma unroll
        for (int i = k; i < 8; ++i) acc[i] = mfma16h(A[it][k], B[it][i - k], acc[i]);
      SB();
    }
    LAS char* yp = smem + A_YP + w * 8192;
#pragma unroll
    for (int i = 0; i < 8; ++i) *(LAS f32x4*)(yp + i * 1024 + bq * 64 + g * 16) = acc[i];
  }
  STAMP(13);
  __syncthreads();
  STAMP(14);
  {
    float y[8];
#pragma unroll
    for (int j = 0; j < 8; ++j) y[j] = 0.f;
#pragma unroll
    for (int j = 0; j < 4; ++j) {
      const LAS char* yp = smem + A_YP + (c2 * 4 + j) * 8192 + t * 32;
      f32x4 a = *(const LAS f32x4*)yp, c = *(const LAS f32x4*)(yp + 16);
#pragma unroll
      for (int e = 0; e < 4; ++e) { y[e] += a[e]; y[4 + e] += c[e]; }
    }
    const float dsk = bf2f(r_dsk);
    u32x4 hv;
#pragma unroll
    for (int j = 0; j < 4; ++j) {
      const unsigned yb = pkbf(y[2 * j] * SKinv, y[2 * j + 1] * SKinv);
      const unsigned sk = pkbf(bflo(uu[j]) * dsk, bfhi(uu[j]) * dsk);
      const unsigned y2 = pkbf(bflo(yb) + bflo(sk), bfhi(yb) + bfhi(sk));
      hv[j] = pkbf(bflo(xx[j]) + bflo(y2), bfhi(xx[j]) + bfhi(y2));
    }
    *(u32x4*)(ws + WS_HT + (size_t)(t >> 3) * (D * 128) + ch * 128 + (t & 7) * 16) = hv;
  }
  STAMP(15);
  STAMP_FLUSH(0);
}

constexpr int M_HT = 0;
constexpr int M_ST = 57344;
constexpr int M_CS = M_ST + 2048;
constexpr int M_P = M_CS + 1024;
constexpr int M_LDS = M_P + 4 * 64 * 272;

__global__ __launch_bounds__(256, 1) void k_mlp(char* __restrict__ ws, unsigned short* __restrict__ out) {
  __shared__ __attribute__((aligned(16))) char smem_[M_LDS];
  LAS char* smem = (LAS char*)smem_;
  const int tid = threadIdx.x, lane = tid & 63;
  const int w = __builtin_amdgcn_readfirstlane(tid >> 6);
  const int xcd = blockIdx.x & 7, idx = blockIdx.x >> 3;
  const int rt = xcd * 4 + idx / 7, j = idx % 7;
  STAMP_DECL
  STAMP(0);
  const u64 tag = load_epoch(ws);
  auto rH = __builtin_amdgcn_make_buffer_rsrc((void*)(ws + WS_HT), 0, L * D * 2, 0x00020000);
  auto rW1 = __builtin_amdgcn_make_buffer_rsrc((void*)(ws + WS_W1F), 0, D * F * 2, 0x00020000);
  auto rW2 = __builtin_amdgcn_make_buffer_rsrc((void*)(ws + WS_W2F), 0, D * F * 2, 0x00020000);
  auto rH1 = __builtin_amdgcn_make_buffer_rsrc((void*)(ws + WS_H1F), 0, L * F * 2, 0x00020000);
  const int l16 = lane * 16;
  u32x4 hv[14];
#pragma unroll
  for (int p = 0; p < 14; ++p) hv[p] = __builtin_amdgcn_raw_buffer_load_b128(rH, tid * 16, rt * (D * 128) + p * 4096, 0);
  SB();
  const int ft0 = j * 8 + w * 2;
  u32x4 wa[2][28];
  auto w1load = [&](int i) {
    const int ks = (((i / 7) + w) & 3) * 7 + (i % 7);
    wa[0][i] = __builtin_amdgcn_raw_buffer_load_b128(rW1, l16, (ft0 * 28 + ks) * 1024, 0);
    wa[1][i] = __builtin_amdgcn_raw_buffer_load_b128(rW1, l16, ((ft0 + 1) * 28 + ks) * 1024, 0);
  };
#pragma unroll
  for (int i = 0; i < 24; ++i) w1load(i);
  SB();
  {
    const int r = tid >> 3, c = tid & 7;
#pragma unroll
    for (int p = 0; p < 14; ++p) { const int row = 32 * p + r; *(LAS u32x4*)(smem + M_HT + row * 128 + ((c ^ (4 * ((row >> 1) & 1))) * 16)) = hv[p]; }
  }
  SB();
  STAMP(1);
  __syncthreads();
  STAMP(2);
  const int hh = lane >> 5, blk = (lane >> 4) & 1, q = (lane & 15) >> 2, p = lane & 3;
  const int sw = (q >> 1) & 1;
  const LAS char* b0 = smem + M_HT + (8 * hh + q) * 128 + ((2 * blk + (p >> 1) + 4 * sw) * 16) + (p & 1) * 8;
  const LAS char* b1 = smem + M_HT + (8 * hh + q) * 128 + ((2 * blk + (p >> 1) + 4 * (1 - sw)) * 16) + (p & 1) * 8;
  f32x16 acc[2][2];
#pragma unroll
  for (int a = 0; a < 2; ++a)
#pragma unroll
    for (int c = 0; c < 2; ++c)
#pragma unroll
      for (int e = 0; e < 16; ++e) acc[a][c][e] = 0.f;
  float s0 = 0.f, s1 = 0.f, q0 = 0.f, q1 = 0.f, cs0 = 0.f, cs1 = 0.f;
  const unsigned ONES = 0x3f803f80u;
  u32x4 wb[2][28];
  auto w2load = [&](int i) {
    const int n = i >> 2, fsub = i & 3;
    int jj = j + n; jj = jj >= 7 ? jj - 7 : jj;
    const int fs = 16 * jj + 4 * w + fsub;
    wb[0][i] = __builtin_amdgcn_raw_buffer_load_b128(rW2, l16, ((2 * j) * 112 + fs) * 1024, 0);
    wb[1][i] = __builtin_amdgcn_raw_buffer_load_b128(rW2, l16, ((2 * j + 1) * 112 + fs) * 1024, 0);
  };
  u32x4 f0, f1, n0, n1;
  auto bload = [&](int i, u32x4& o0, u32x4& o1) {
    const int go = ((((i / 7) + w) & 3) * 7 + (i % 7)) * 2048;
    o0 = tr_read2(b0 + go, b0 + go + 512);
    o1 = tr_read2(b1 + go, b1 + go + 512);
  };
  bload(0, f0, f1);
#pragma unroll
  for (int i = 0; i < 28; ++i) {
    if (i + 1 < 28) bload(i + 1, n0, n1);
    if (i + 24 < 28) w1load(i + 24); else if (i < 12) w2load(i - 4);
    SB();
    acc[0][0] = mfma32b(wa[0][i], f0, acc[0][0]);
    acc[0][1] = mfma32b(wa[0][i], f1, acc[0][1]);
    acc[1][0] = mfma32b(wa[1][i], f0, acc[1][0]);
    acc[1][1] = mfma32b(wa[1][i], f1, acc[1][1]);
#pragma unroll
    for (int e = 0; e < 4; ++e) { cs0 = dot2bf(wa[0][i][e], ONES, cs0); cs1 = dot2bf(wa[1][i][e], ONES, cs1); }
    if (i < 7) {
#pragma unroll
      for (int e = 0; e < 4; ++e) { s0 = dot2bf(f0[e], ONES, s0); q0 = dot2bf(f0[e], f0[e], q0); s1 = dot2bf(f1[e], ONES, s1); q1 = dot2bf(f1[e], f1[e], q1); }
    }
    SB();
    f0 = n0; f1 = n1;
  }
  STAMP(3);
  s0 += __shfl_xor(s0, 32); s1 += __shfl_xor(s1, 32); q0 += __shfl_xor(q0, 32); q1 += __shfl_xor(q1, 32);
  cs0 += __shfl_xor(cs0, 32); cs1 += __shfl_xor(cs1, 32);
  if (hh == 0) {
    *(LAS u32x2*)(smem + M_ST + (w * 64 + lane) * 8) = u32x2{__float_as_uint(s0), __float_as_uint(q0)};
    *(LAS u32x2*)(smem + M_ST + (w * 64 + 32 + lane) * 8) = u32x2{__float_as_uint(s1), __float_as_uint(q1)};
    *(LAS float*)(smem + M_CS + (w * 64 + lane) * 4) = cs0;
    *(LAS float*)(smem + M_CS + (w * 64 + 32 + lane) * 4) = cs1;
  }
  __syncthreads();
  STAMP(4);
  float mr[2], ri[2];
#pragma unroll
  for (int c = 0; c < 2; ++c) {
    float s = 0.f, qq = 0.f;
#pragma unroll
    for (int ww = 0; ww < 4; ++ww) { u32x2 v = *(const LAS u32x2*)(smem + M_ST + (ww * 64 + c * 32 + (lane & 31)) * 8); s += __uint_as_float(v[0]); qq += __uint_as_float(v[1]); }
    const float mean = s * (1.0f / D);
    const float var = fmaxf(qq * (1.0f / D) - mean * mean, 0.f);
    float mb; ln_scalars(mean, var, mb, ri[c]);
    mr[c] = mb;
  }
  u32x4 own[2][4];
#pragma unroll
  for (int a = 0; a < 2; ++a) {
    float cs[16];
#pragma unroll
    for (int g4 = 0; g4 < 4; ++g4) {
      f32x4 v = *(const LAS f32x4*)(smem + M_CS + (w * 64 + a * 32 + 8 * g4 + 4 * hh) * 4);
#pragma unroll
      for (int e = 0; e < 4; ++e) cs[4 * g4 + e] = v[e];
    }
#pragma unroll
    for (int c = 0; c < 2; ++c) {
#pragma unroll
      for (int s = 0; s < 2; ++s) {
        u32x4 fr;
#pragma unroll
        for (int e = 0; e < 4; ++e) {
          const int r0 = 8 * s + 2 * e;
          const float v0 = (acc[a][c][r0] - mr[c] * cs[r0]) * ri[c], v1 = (acc[a][c][r0 + 1] - mr[c] * cs[r0 + 1]) * ri[c];
          fr[e] = pkbf(fmaxf(v0, 0.f), fmaxf(v1, 0.f));
        }
        own[c][2 * a + s] = fr;
        const int fs = 16 * j + 4 * w + 2 * a + s;
        __builtin_amdgcn_raw_buffer_store_b128(fr, rH1, l16, ((rt * 2 + c) * 112 + fs) * 1024, 16);
      }
    }
  }
  SB();
#pragma unroll
  for (int i = 8; i < 28; ++i) w2load(i);
  SB();
  asm volatile("s_waitcnt vmcnt(40)" ::: "memory");
  GAS u64* flg = (GAS u64*)(ws + WS_FLAGB) + (rt * 4 + w) * 8;
  if (lane == 0) __hip_atomic_store(flg + j, tag, __ATOMIC_RELAXED, __HIP_MEMORY_SCOPE_AGENT);
  STAMP(5);
#pragma unroll
  for (int a = 0; a < 2; ++a)
#pragma unroll
    for (int c = 0; c < 2; ++c)
#pragma unroll
      for (int e = 0; e < 16; ++e) acc[a][c][e] = 0.f;
#pragma unroll
  for (int i = 0; i < 4; ++i) {
    acc[0][0] = mfma32b(wb[0][i], own[0][i], acc[0][0]);
    acc[0][1] = mfma32b(wb[0][i], own[1][i], acc[0][1]);
    acc[1][0] = mfma32b(wb[1][i], own[0][i], acc[1][0]);
    acc[1][1] = mfma32b(wb[1][i], own[1][i], acc[1][1]);
  }
  SB();
  {
    const int fl = lane < 7 ? lane : 0;
    for (unsigned spins = 0;; ++spins) {
      const u64 v = __hip_atomic_load(flg + fl, __ATOMIC_RELAXED, __HIP_MEMORY_SCOPE_AGENT);
      if (__all(v == tag)) break;
      if (spins > kSpinCap) { if (lane == 0) __hip_atomic_store((GAS unsigned*)(ws + WS_TMO), 2u, __ATOMIC_RELAXED, __HIP_MEMORY_SCOPE_AGENT); break; }
      __builtin_amdgcn_s_sleep(1);
    }
  }
  asm volatile("" ::: "memory");
  STAMP(6);
  u32x4 hf[2][24];
#pragma unroll
  for (int i = 4; i < 28; ++i) {
    const int n = i >> 2, fsub = i & 3;
    int jj = j + n; jj = jj >= 7 ? jj - 7 : jj;
    const int fs = 16 * jj + 4 * w + fsub;
    hf[0][i - 4] = __builtin_amdgcn_raw_buffer_load_b128(rH1, l16, ((rt * 2) * 112 + fs) * 1024, 16);
    hf[1][i - 4] = __builtin_amdgcn_raw_buffer_load_b128(rH1, l16, ((rt * 2 + 1) * 112 + fs) * 1024, 16);
  }
  SB();
#pragma unroll
  for (int i = 4; i < 28; ++i) {
    acc[0][0] = mfma32b(wb[0][i], hf[0][i - 4], acc[0][0]);
    acc[0][1] = mfma32b(wb[0][i], hf[1][i - 4], acc[0][1]);
    acc[1][0] = mfma32b(wb[1][i], hf[0][i - 4], acc[1][0]);
    acc[1][1] = mfma32b(wb[1][i], hf[1][i - 4], acc[1][1]);
    SB();
  }
  STAMP(7);
#pragma unroll
  for (int a = 0; a < 2; ++a)
#pragma unroll
    for (int c = 0; c < 2; ++c)
#pragma unroll
      for (int g4 = 0; g4 < 4; ++g4) {
        f32x4 v = {acc[a][c][4 * g4], acc[a][c][4 * g4 + 1], acc[a][c][4 * g4 + 2], acc[a][c][4 * g4 + 3]};
        *(LAS f32x4*)(smem + M_P + (w * 64 + c * 32 + (lane & 31)) * 272 + (a * 32 + 8 * g4 + 4 * hh) * 4) = v;
      }
  __syncthreads();
  STAMP(8);
  {
    const int l = lane, c4 = w;
    float o[16];
#pragma unroll
    for (int e = 0; e < 16; ++e) o[e] = 0.f;
#pragma unroll
    for (int ww = 0; ww < 4; ++ww)
#pragma unroll
      for (int i = 0; i < 4; ++i) {
        f32x4 v = *(const LAS f32x4*)(smem + M_P + (ww * 64 + l) * 272 + (16 * c4 + 4 * i) * 4);
#pragma unroll
        for (int e = 0; e < 4; ++e) o[4 * i + e] += v[e];
      }
    unsigned pk[8];
#pragma unroll
    for (int e = 0; e < 8; ++e) {
      const int r0 = 64 * j + 16 * c4 + 2 * e, r1 = r0 + 1;
      const float h0 = bf2f(*(const LAS unsigned short*)(smem + M_HT + r0 * 128 + (((l >> 3) ^ (4 * ((r0 >> 1) & 1))) * 16) + (l & 7) * 2));
      const float h1 = bf2f(*(const LAS unsigned short*)(smem + M_HT + r1 * 128 + (((l >> 3) ^ (4 * ((r1 >> 1) & 1))) * 16) + (l & 7) * 2));
      const unsigned ob = pkbf(o[2 * e], o[2 * e + 1]);
      pk[e] = pkbf(h0 + bflo(ob), h1 + bfhi(ob));
    }
    char* op = (char*)out + ((size_t)(rt * 64 + l) * D + 64 * j + 16 * c4) * 2;
    *(u32x4*)op = u32x4{pk[0], pk[1], pk[2], pk[3]};
    *(u32x4*)(op + 16) = u32x4{pk[4], pk[5], pk[6], pk[7]};
  }
  STAMP(9);
  STAMP_FLUSH(1);
}

}

extern "C" void kernel_launch(void* const* d_in, const int* in_sizes, int n_in, void* d_out, int out_size, void* d_ws, size_t ws_size, hipStream_t stream) {
  typedef const unsigned short* cp;
  k_ssm<<<256, 512, 0, stream>>>((cp)d_in[0], (cp)d_in[1], (cp)d_in[2], (cp)d_in[3], (cp)d_in[4], (cp)d_in[5], (cp)d_in[6], (cp)d_in[7], (cp)d_in[8], (cp)d_in[10], (cp)d_in[11], (char*)d_ws);
  k_mlp<<<224, 256, 0, stream>>>((char*)d_ws, (unsigned short*)d_out);
}
```

```cpp
#include <hip/hip_runtime.h>
#include <stdint.h>

namespace {

typedef __attribute__((ext_vector_type(8))) _Float16 f16x8;
typedef __attribute__((ext_vector_type(4))) _Float16 f16x4;
typedef __attribute__((ext_vector_type(2))) _Float16 f16x2;
typedef __attribute__((ext_vector_type(8))) __bf16 bf16x8;
typedef __attribute__((ext_vector_type(2))) __bf16 bf16x2;
typedef __attribute__((ext_vector_type(4))) float f32x4;
typedef __attribute__((ext_vector_type(16))) float f32x16;
typedef __attribute__((ext_vector_type(4))) unsigned u32x4;
typedef __attribute__((ext_vector_type(2))) unsigned u32x2;
typedef __attribute__((ext_vector_type(4))) short s16x4;
typedef unsigned long long u64;
#define LAS __attribute__((address_space(3)))
#define GAS __attribute__((address_space(1)))
#define DI __device__ __forceinline__

constexpr int L = 2048, D = 448, NM = 256, F = 1792;

constexpr size_t WS_EPOCH = 0;
constexpr size_t WS_TMO = 64;
constexpr size_t WS_FLAGB = 8192;
constexpr size_t WS_STATS = 16384;
constexpr size_t WS_FX = WS_STATS + 2048 * 16;
constexpr size_t WS_XT = WS_FX + 7 * 256 * 8 + 2048;
constexpr size_t WS_CS = WS_XT + (size_t)L * D * 2;
constexpr size_t WS_HT = WS_CS + 4 * F * 4;
constexpr size_t WS_W1F = WS_HT + (size_t)D * L * 2;
constexpr size_t WS_W2F = WS_W1F + (size_t)D * F * 2;
constexpr size_t WS_H1F = WS_W2F + (size_t)D * F * 2;
constexpr size_t WS_END = WS_H1F + (size_t)L * F * 2;
constexpr size_t WS_STAMP = WS_END;
constexpr unsigned kSpinCap = 1u << 20;

#define STAMP(i) do {} while (0)
#define STAMP_DECL
#define STAMP_FLUSH(k) do {} while (0)
#define SB() __builtin_amdgcn_sched_barrier(0)
DI float bf2f(unsigned b) { return __uint_as_float(b << 16); }
DI float bflo(unsigned p) { return __uint_as_float(p << 16); }
DI float bfhi(unsigned p) { return __uint_as_float(p & 0xffff0000u); }
DI unsigned pkbf(float a, float b) { bf16x2 r; r[0] = (__bf16)a; r[1] = (__bf16)b; return __builtin_bit_cast(unsigned, r); }
DI float rbf(float a) { return bflo(pkbf(a, a)); }
DI unsigned pkh(float a, float b) { f16x2 r; r[0] = (_Float16)a; r[1] = (_Float16)b; return __builtin_bit_cast(unsigned, r); }

template <int CTRL> DI float dppf(float v) { return __int_as_float(__builtin_amdgcn_update_dpp(0, __float_as_int(v), CTRL, 0xF, 0xF, true)); }
DI float rdl(float v, int l) { return __int_as_float(__builtin_amdgcn_readlane(__float_as_int(v), l)); }
DI float wave_sum(float v) {
  v += dppf<0xB1>(v); v += dppf<0x4E>(v); v += dppf<0x141>(v); v += dppf<0x140>(v);
  return (rdl(v, 0) + rdl(v, 16)) + (rdl(v, 32) + rdl(v, 48));
}
DI float wave_max(float v) {
  v = fmaxf(v, dppf<0xB1>(v)); v = fmaxf(v, dppf<0x4E>(v)); v = fmaxf(v, dppf<0x141>(v)); v = fmaxf(v, dppf<0x140>(v));
  return fmaxf(fmaxf(rdl(v, 0), rdl(v, 16)), fmaxf(rdl(v, 32), rdl(v, 48)));
}
DI s16x4 tr_read(const LAS char* p) { return __builtin_amdgcn_ds_read_tr16_b64_v4i16((LAS s16x4*)p); }
DI u32x4 tr_read2(const LAS char* p0, const LAS char* p1) {
  s16x4 a = tr_read(p0), b = tr_read(p1);
  u32x2 a2 = __builtin_bit_cast(u32x2, a), b2 = __builtin_bit_cast(u32x2, b);
  u32x4 r; r[0] = a2[0]; r[1] = a2[1]; r[2] = b2[0]; r[3] = b2[1]; return r;
}
DI f32x4 mfma16h(u32x4 a, u32x4 b, f32x4 c) { return __builtin_amdgcn_mfma_f32_16x16x32_f16(__builtin_bit_cast(f16x8, a), __builtin_bit_cast(f16x8, b), c, 0, 0, 0); }
DI f32x16 mfma32b(u32x4 a, u32x4 b, f32x16 c) { return __builtin_amdgcn_mfma_f32_32x32x16_bf16(__builtin_bit_cast(bf16x8, a), __builtin_bit_cast(bf16x8, b), c, 0, 0, 0); }
DI float dot2bf(unsigned a, unsigned b, float c) { return __builtin_amdgcn_fdot2_f32_bf16(__builtin_bit_cast(bf16x2, a), __builtin_bit_cast(bf16x2, b), c, false); }

DI u64 load_epoch(const char* ws) {
  const u32x2 v = *(const u32x2*)(ws + WS_EPOCH);
  unsigned lo = __builtin_amdgcn_readfirstlane(v[0]), hi = __builtin_amdgcn_readfirstlane(v[1]);
  asm volatile("" : "+s"(lo), "+s"(hi));
  return ((u64)hi << 32) | lo;
}
struct cf { float r, i; };
DI cf cmul(cf a, cf b) { cf o; o.r = a.r * b.r - a.i * b.i; o.i = a.r * b.i + a.i * b.r; return o; }
DI cf csq(cf a) { cf o; o.r = a.r * a.r - a.i * a.i; o.i = 2.f * a.r * a.i; return o; }
DI cf cexp_p2(float zr2, float zf, float p2) {
  float m = __builtin_amdgcn_exp2f(zr2 * p2);
  float t = __builtin_amdgcn_fractf(zf * p2);
  cf o; o.r = m * __builtin_amdgcn_cosf(t); o.i = m * __builtin_amdgcn_sinf(t); return o;
}

DI void ln_scalars(float mean, float var, float& mb, float& sb) {
  mb = rbf(mean);
  float vb = rbf(var);
  sb = rbf(__builtin_sqrtf(rbf(vb + 1e-5f)));
}
DI float rcp_(float s) { return __builtin_amdgcn_rcpf(s); }

struct IdxTab { unsigned short v[256][12]; };
constexpr int bf16_round_index(int lag) {
  if (lag < 0 || lag > 2047) return 641;
  if (lag < 256) return lag;
  int e = 8; while ((lag >> (e + 1)) != 0) ++e;
  const int sh = e - 7;
  int q = lag >> sh; const int r = lag & ((1 << sh) - 1), half = 1 << (sh - 1);
  if (r > half || (r == half && (q & 1))) ++q;
  return 256 + (e - 8) * 128 + (q - 128);
}
constexpr IdxTab make_idx() {
  IdxTab t{};
  for (int c = 0; c < 256; ++c) { for (int e = 0; e < 11; ++e) t.v[c][e] = (unsigned short)(2 * bf16_round_index(2048 - 8 * c + 3 - e)); t.v[c][11] = 0; }
  return t;
}
__device__ const IdxTab g_idx = make_idx();

constexpr int A_TAB = 0;
constexpr int A_MISC = A_TAB + 8 * 16384;
constexpr int A_KDC = A_MISC;
constexpr int A_SC = A_KDC + 2 * 1312;
constexpr int A_LDS = A_SC + 128;
constexpr int TOE_CP = 4160, TOE_CH = 4 * TOE_CP;
constexpr int A_U16 = A_TAB;
constexpr int A_TOE2 = A_TAB + 9216;
constexpr int A_YP = A_TAB + 42496;

__global__ __launch_bounds__(512, 2) void k_ssm(const unsigned short* __restrict__ x, const unsigned short* __restrict__ Are, const unsigned short* __restrict__ Aim,
                                                 const unsigned short* __restrict__ Bre, const unsigned short* __restrict__ Bim, const unsigned short* __restrict__ Cre,
                                                 const unsigned short* __restrict__ Cim, const unsigned short* __restrict__ logdt, const unsigned short* __restrict__ Dsk,
                                                 const unsigned short* __restrict__ w1, const unsigned short* __restrict__ w2, char* __restrict__ ws) {
  __shared__ __attribute__((aligned(16))) char smem_[A_LDS];
  LAS char* smem = (LAS char*)smem_;
  const int tid = threadIdx.x, lane = tid & 63;
  const int w = __builtin_amdgcn_readfirstlane(tid >> 6);
  const int b = blockIdx.x;
  const bool has_ch = b < 224;
  const int c2 = w >> 2, w4 = w & 3;
  const int ch = 2 * b + c2;
  STAMP_DECL
  STAMP(0);
  const int row = 8 * b + w;
  u32x4 xv = *(const u32x4*)(x + (size_t)row * D + min(lane, 55) * 8);
  const int bs = has_ch ? b : 0;
  const int trt = bs & 31, tg = bs >> 5;
  const u32x4 xt = *(const u32x4*)(x + (size_t)(64 * trt + 8 * w + (lane >> 3)) * D + 64 * tg + (lane & 7) * 8);
  const u32x2 ev = *(const u32x2*)(ws + WS_EPOCH);
  const int chs = 2 * bs + c2;
  const int po = chs * NM + w4 * 64 + lane;
  const unsigned short r_are = Are[po], r_aim = Aim[po], r_bre = Bre[po], r_bim = Bim[po], r_cre = Cre[po], r_cim = Cim[po];
  const unsigned short r_ldt = logdt[chs];
  const unsigned r_dsk = *(const unsigned*)(Dsk + 2 * bs);
  const int t = tid & 255;
  const u32x4 ix0 = *(const u32x4*)&g_idx.v[t][0];
  const u32x2 ix1 = *(const u32x2*)&g_idx.v[t][8];
  SB();
  if (lane >= 56) xv = u32x4{0u, 0u, 0u, 0u};
  unsigned elo = __builtin_amdgcn_readfirstlane(ev[0]), ehi = __builtin_amdgcn_readfirstlane(ev[1]);
  asm volatile("" : "+s"(elo), "+s"(ehi));
  const u64 tag = (((u64)ehi << 32) | elo) + 1;
  const unsigned tlo_ = (unsigned)tag, thi_ = (unsigned)(tag >> 32);

  {
    float xf[8];
#pragma unroll
    for (int j = 0; j < 4; ++j) { xf[2 * j] = bflo(xv[j]); xf[2 * j + 1] = bfhi(xv[j]); }
    float s = 0.f;
#pragma unroll
    for (int j = 0; j < 8; ++j) s += xf[j];
    const float mean = wave_sum(s) * (1.0f / D);
    float q = 0.f;
#pragma unroll
    for (int j = 0; j < 8; ++j) { float d = xf[j] - mean; q += d * d; }
    if (lane >= 56) q = 0.f;
    const float var = wave_sum(q) * (1.0f / D);
    float mb, sb; ln_scalars(mean, var, mb, sb);
    const unsigned pay = (__float_as_uint(mb) >> 16) | (__float_as_uint(sb) & 0xffff0000u);
    auto rs = __builtin_amdgcn_make_buffer_rsrc((void*)(ws + WS_STATS), 0, 2048 * 16, 0x00020000);
    if (lane == 0) __builtin_amdgcn_raw_buffer_store_b128(u32x4{tlo_, pay, thi_, pay}, rs, row * 16, 0, 16);
  }
  STAMP(1);

  if (!has_ch) {
    const int task = (b - 224) * 8 + w;
    if (task < 224) {
      const bool is_w1 = task < 112;
      const int tk = is_w1 ? task : task - 112;
      const int nb = is_w1 ? tk % 28 : tk % 7, kg = is_w1 ? tk / 28 : tk / 7;
      const int rld = is_w1 ? F : D;
      const unsigned short* src = (is_w1 ? w1 : w2) + (size_t)(16 * 7 * kg + (lane >> 3)) * rld + 64 * nb + (lane & 7) * 8;
      u32x4 r0[7], r1[7];
#pragma unroll
      for (int u = 0; u < 7; ++u) { r0[u] = *(const u32x4*)(src + (size_t)(16 * u) * rld); r1[u] = *(const u32x4*)(src + (size_t)(16 * u + 8) * rld); }
      LAS char* sc = smem + w * 16384;
#pragma unroll
      for (int u = 0; u < 7; ++u) { *(LAS u32x4*)(sc + u * 2048 + lane * 16) = r0[u]; *(LAS u32x4*)(sc + u * 2048 + 1024 + lane * 16) = r1[u]; }
      __builtin_amdgcn_s_waitcnt(0xC07F);
      __builtin_amdgcn_wave_barrier();
      const int g = lane >> 4, blk = g & 1, hh = g >> 1, q = (lane & 15) >> 2, p = lane & 3;
      const int q0 = is_w1 ? (8 * hh + q) : (4 * hh + q);
      const int q1 = is_w1 ? (8 * hh + 4 + q) : (8 + 4 * hh + q);
      auto rd = __builtin_amdgcn_make_buffer_rsrc((void*)(ws + (is_w1 ? WS_W1F : WS_W2F)), 0, D * F * 2, 0x00020000);
      const int nks = is_w1 ? 28 : 112;
      float cs[2] = {0.f, 0.f};
#pragma unroll
      for (int u = 0; u < 7; ++u) {
#pragma unroll
        for (int tt = 0; tt < 2; ++tt) {
          const int cb = (tt * 32 + blk * 16 + 4 * p) * 2;
          u32x4 fr = tr_read2(sc + u * 2048 + q0 * 128 + cb, sc + u * 2048 + q1 * 128 + cb);
          __builtin_amdgcn_raw_buffer_store_b128(fr, rd, lane * 16, ((2 * nb + tt) * nks + 7 * kg + u) * 1024, 16);
#pragma unroll
          for (int e = 0; e < 4; ++e) cs[tt] = dot2bf(fr[e], 0x3f803f80u, cs[tt]);
        }
      }
      if (is_w1) {
        cs[0] += __shfl_xor(cs[0], 32); cs[1] += __shfl_xor(cs[1], 32);
        auto rc = __builtin_amdgcn_make_buffer_rsrc((void*)(ws + WS_CS), 0, 4 * F * 4, 0x00020000);
        __builtin_amdgcn_raw_buffer_store_b32(__float_as_uint(lane < 32 ? cs[0] : cs[1]), rc, (kg * F + 64 * nb + lane) * 4, 0, 16);
      }
    }
    return;
  }

  LAS char* tab = smem + A_TAB + w * 16384;
  {
    *(LAS u32x4*)(tab + lane * 16) = xt;
    __builtin_amdgcn_s_waitcnt(0xC07F);
    __builtin_amdgcn_wave_barrier();
    const int g = lane >> 4, q = (lane & 15) >> 2, p = lane & 3;
    const LAS char* a0 = tab + q * 128 + (16 * g + 4 * p) * 2;
    const s16x4 lo = tr_read(a0), hi = tr_read(a0 + 512);
    auto rx = __builtin_amdgcn_make_buffer_rsrc((void*)(ws + WS_XT), 0, L * D * 2, 0x00020000);
    const int o = ((trt * 16 + 2 * w) * D + 64 * tg + lane) * 8;
    __builtin_amdgcn_raw_buffer_store_b64(__builtin_bit_cast(u32x2, lo), rx, o, 0, 16);
    __builtin_amdgcn_raw_buffer_store_b64(__builtin_bit_cast(u32x2, hi), rx, o + D * 8, 0, 16);
    __builtin_amdgcn_s_waitcnt(0xC07F);
    __builtin_amdgcn_wave_barrier();
  }
  STAMP(13);
  float k2048;
  int ew;
  {
    const float are = bf2f(r_are), aim = bf2f(r_aim), bre = bf2f(r_bre), bim = bf2f(r_bim), cre = bf2f(r_cre), cim = bf2f(r_cim);
    const float dt = rbf(expf(bf2f(r_ldt)));
    const float zr = dt * are, zi = dt * aim;
    const float zr2 = zr * 1.44269504088896341f, zf = zi * 0.15915494309189535f;
    const float dbr = dt * bre, dbi = dt * bim;
    cf CB; CB.r = cre * dbr - cim * dbi; CB.i = cre * dbi + cim * dbr;
    const float mx = wave_max(fmaxf(fabsf(CB.r), fabsf(CB.i)));
    const float gz = fmaxf(wave_max(zr2), 0.f) * 2048.f;
    int e = (mx > 0.f) ? ((int)(__float_as_uint(mx) >> 23) - 127) : 0;
    e += (int)ceilf(fminf(gz, 100.f));
    e = max(-120, min(120, e));
    ew = e;
    const float S = __uint_as_float((unsigned)(127 - e) << 23);
    cf G0; G0.r = CB.r * S; G0.i = CB.i * S;
    const cf E1 = cexp_p2(zr2, zf, 1.f), E2 = csq(E1), E4 = cexp_p2(zr2, zf, 4.f), E8 = csq(E4);
    const cf E16 = cexp_p2(zr2, zf, 16.f), E32 = csq(E16), E64 = cexp_p2(zr2, zf, 64.f), E128 = csq(E64);
    const cf E256 = cexp_p2(zr2, zf, 256.f), E512 = csq(E256), E1024 = cexp_p2(zr2, zf, 1024.f), E2048 = csq(E1024);
    STAMP(14);
    const int sw = (lane >> 1) & 3;
    LAS char* tb = tab + lane * 64;
    auto put = [&](int tile, int half, const cf* v, float isign) {
      u32x4 re, im;
#pragma unroll
      for (int j = 0; j < 4; ++j) { re[j] = pkh(v[2 * j].r, v[2 * j + 1].r); im[j] = pkh(isign * v[2 * j].i, isign * v[2 * j + 1].i); }
      *(LAS u32x4*)(tb + tile * 4096 + ((half ^ sw) * 16)) = re;
      *(LAS u32x4*)(tb + tile * 4096 + (((2 + half) ^ sw) * 16)) = im;
    };
    cf Er[8];
    Er[0].r = 1.f; Er[0].i = 0.f; Er[1] = E1; Er[2] = E2; Er[3] = cmul(E1, E2); Er[4] = E4; Er[5] = cmul(E4, E1); Er[6] = cmul(E4, E2); Er[7] = cmul(E4, Er[3]);
    {
      cf Y[8];
      Y[0].r = 1.f; Y[0].i = 0.f; Y[1] = E8; Y[2] = E16; Y[3] = cmul(E8, E16); Y[4] = E32; Y[5] = cmul(E32, E8); Y[6] = cmul(E32, E16); Y[7] = cmul(E32, Y[3]);
      put(3, 0, Y, 1.f);
      cf Y2[8];
      Y2[0] = E64;
#pragma unroll
      for (int j = 1; j < 8; ++j) Y2[j] = cmul(Y[j], E64);
      put(3, 1, Y2, 1.f);
    }
    STAMP(15);
    cf G[8];
    G[0] = G0; G[1] = cmul(G0, E128); G[2] = cmul(G0, E256); G[3] = cmul(G[1], E256);
#pragma unroll
    for (int j = 0; j < 4; ++j) G[4 + j] = cmul(G[j], E512);
    {
      cf T[8];
      T[0] = G[0];
#pragma unroll
      for (int j = 1; j < 8; ++j) T[j] = cmul(G[0], Er[j]);
      put(0, 0, T, -1.f);
      T[0] = G[1];
#pragma unroll
      for (int j = 1; j < 8; ++j) T[j] = cmul(G[1], Er[j]);
      put(0, 1, T, -1.f);
    }
    SB();
    asm volatile("s_waitcnt vmcnt(0)" ::: "memory");
    if (lane == 0) __hip_atomic_store((GAS u64*)(ws + WS_FX) + tg * 256 + trt * 8 + w, tag, __ATOMIC_RELAXED, __HIP_MEMORY_SCOPE_AGENT);
    SB();
    {
      cf T[8];
      T[0] = G[2]; T[1] = cmul(G[2], E2); T[2] = cmul(G[2], E4); T[3] = cmul(G[2], Er[6]);
      T[4] = G[3]; T[5] = cmul(G[3], E2); T[6] = cmul(G[3], E4); T[7] = cmul(G[3], Er[6]);
      put(1, 0, T, -1.f);
#pragma unroll
      for (int j = 0; j < 4; ++j) { T[2 * j] = G[4 + j]; T[2 * j + 1] = cmul(G[4 + j], E4); }
      put(1, 1, T, -1.f);
    }
    {
      cf T[8];
#pragma unroll
      for (int j = 0; j < 8; ++j) T[j] = cmul(G[j], E1024);
      put(2, 0, T, -1.f);
    }
    k2048 = wave_sum(G0.r * E2048.r - G0.i * E2048.i);
  }
  STAMP(2);
  __builtin_amdgcn_s_waitcnt(0xC07F);
  __builtin_amdgcn_wave_barrier();
  {
    const int g = lane >> 4, q = (lane & 15) >> 2, p = lane & 3;
    f32x4 acc0 = {0.f, 0.f, 0.f, 0.f}, acc1 = acc0, acc2 = acc0;
    const int part = q & 1, cl = part * 2 + (p >> 1);
    u32x4 bb[4], a0[4], a1[4], a2[4];
#pragma unroll
    for (int s = 0; s < 4; ++s) {
      const int m0 = 16 * s + 8 * (g >> 1) + 2 * (g & 1) + (q >> 1), m1 = m0 + 4;
      const int o0 = m0 * 64 + ((cl ^ ((m0 >> 1) & 3)) * 16) + (p & 1) * 8;
      const int o1 = m1 * 64 + ((cl ^ ((m1 >> 1) & 3)) * 16) + (p & 1) * 8;
      bb[s] = tr_read2(tab + 3 * 4096 + o0, tab + 3 * 4096 + o1);
      a0[s] = tr_read2(tab + o0, tab + o1);
      a1[s] = tr_read2(tab + 4096 + o0, tab + 4096 + o1);
      a2[s] = tr_read2(tab + 8192 + o0, tab + 8192 + o1);
    }
    SB();
#pragma unroll
    for (int s = 0; s < 4; ++s) { acc0 = mfma16h(a0[s], bb[s], acc0); acc1 = mfma16h(a1[s], bb[s], acc1); acc2 = mfma16h(a2[s], bb[s], acc2); }
    SB();
    *(LAS f32x4*)(tab + lane * 16) = acc0;
    *(LAS f32x4*)(tab + 1024 + lane * 16) = acc1;
    *(LAS f32x4*)(tab + 2048 + lane * 16) = acc2;
    if (lane == 0) { *(LAS int*)(smem + A_SC + w * 8) = ew; *(LAS float*)(smem + A_SC + w * 8 + 4) = k2048; }
  }
  STAMP(3);
  auto rst = __builtin_amdgcn_make_buffer_rsrc((void*)(ws + WS_STATS), 0, 2048 * 16, 0x00020000);
  u32x4 gr[4];
#pragma unroll
  for (int i = 0; i < 4; ++i) gr[i] = __builtin_amdgcn_raw_buffer_load_b128(rst, (4 * tid + i) * 16, 0, 16);
  const GAS u64* fxp = (const GAS u64*)(ws + WS_FX) + (ch >> 6) * 256 + (tid >> 1);
  u64 fxv = __hip_atomic_load(fxp, __ATOMIC_RELAXED, __HIP_MEMORY_SCOPE_AGENT);
  __syncthreads();
  STAMP(4);
  float SKinv;
  {
    int e4[4]; float k4[4];
#pragma unroll
    for (int j = 0; j < 4; ++j) { e4[j] = *(LAS int*)(smem + A_SC + (c2 * 4 + j) * 8); k4[j] = *(LAS float*)(smem + A_SC + (c2 * 4 + j) * 8 + 4); }
    const int em = max(max(e4[0], e4[1]), max(e4[2], e4[3]));
    float fw[4];
#pragma unroll
    for (int j = 0; j < 4; ++j) { int d = max(e4[j] - em + 5, -120); fw[j] = __uint_as_float((unsigned)(127 + d) << 23); }
    SKinv = __uint_as_float((unsigned)(127 + em - 5) << 23);
    LAS char* kdc = smem + A_KDC + c2 * 1312;
    if (t < 192) {
      const int tile = t >> 6, ln = t & 63, c = ln & 15, g = ln >> 4;
      f32x4 s = {0.f, 0.f, 0.f, 0.f};
#pragma unroll
      for (int j = 0; j < 4; ++j) { f32x4 v = *(LAS f32x4*)(smem + A_TAB + (c2 * 4 + j) * 16384 + tile * 1024 + ln * 16); s += v * fw[j]; }
      const unsigned p01 = pkh(s[0], s[1]), p23 = pkh(s[2], s[3]);
      if (tile == 0) {
        u32x2 v = {p01, p23}; *(LAS u32x2*)(kdc + (128 * (g >> 1) + 8 * c + 4 * (g & 1)) * 2) = v;
      } else if (tile == 1) {
        if (g < 2) { u32x2 v = {p01, p23}; *(LAS u32x2*)(kdc + (256 + 64 * g + 4 * c) * 2) = v; }
        else { const int i0 = 384 + 64 * (g - 2) + 2 * c; *(LAS unsigned*)(kdc + i0 * 2) = p01; *(LAS unsigned*)(kdc + (i0 + 32) * 2) = p23; }
      } else if (g < 2) {
        const int i0 = 512 + 64 * g + c;
        *(LAS unsigned short*)(kdc + i0 * 2) = (unsigned short)p01; *(LAS unsigned short*)(kdc + (i0 + 16) * 2) = (unsigned short)(p01 >> 16);
        *(LAS unsigned short*)(kdc + (i0 + 32) * 2) = (unsigned short)p23; *(LAS unsigned short*)(kdc + (i0 + 48) * 2) = (unsigned short)(p23 >> 16);
      }
    } else if (t == 192) {
      const float k = k4[0] * fw[0] + k4[1] * fw[1] + k4[2] * fw[2] + k4[3] * fw[3];
      *(LAS unsigned*)(kdc + 640 * 2) = pkh(k, 0.f);
    }
  }
  float SKo;
  {
    int e4[4];
#pragma unroll
    for (int j = 0; j < 4; ++j) e4[j] = *(LAS int*)(smem + A_SC + ((1 - c2) * 4 + j) * 8);
    const int em = max(max(e4[0], e4[1]), max(e4[2], e4[3]));
    SKo = __uint_as_float((unsigned)(127 + em - 5) << 23);
  }
  const float SK0 = c2 ? SKo : SKinv, SK1 = c2 ? SKinv : SKo;
  u32x4 xq;
  {
    for (unsigned spins = 0;; ++spins) {
      if (__all(fxv == tag)) break;
      if (spins > kSpinCap) { if (lane == 0) __hip_atomic_store((GAS unsigned*)(ws + WS_TMO), 3u, __ATOMIC_RELAXED, __HIP_MEMORY_SCOPE_AGENT); break; }
      __builtin_amdgcn_s_sleep(1);
      fxv = __hip_atomic_load(fxp, __ATOMIC_RELAXED, __HIP_MEMORY_SCOPE_AGENT);
    }
    asm volatile("" ::: "memory");
    auto rx = __builtin_amdgcn_make_buffer_rsrc((void*)(ws + WS_XT), 0, L * D * 2, 0x00020000);
    xq = __builtin_amdgcn_raw_buffer_load_b128(rx, (tid * D + 2 * b) * 8, 0, 16);
  }
  STAMP(5);
  __syncthreads();
  STAMP(6);
  {
    const LAS char* kdc = smem + A_KDC + c2 * 1312;
    LAS char* toe = smem + A_TOE2 + c2 * TOE_CH;
    unsigned v[11];
#pragma unroll
    for (int e = 0; e < 11; ++e) {
      const unsigned wd = e < 8 ? ix0[e >> 1] : ix1[(e - 8) >> 1];
      const unsigned off = (e & 1) ? (wd >> 16) : (wd & 0xffffu);
      v[e] = *(const LAS unsigned short*)(kdc + off);
    }
    unsigned ev_[5], od[5];
#pragma unroll
    for (int j = 0; j < 5; ++j) { ev_[j] = v[2 * j] | (v[2 * j + 1] << 16); od[j] = v[2 * j + 1] | (v[2 * j + 2] << 16); }
    *(LAS u32x4*)(toe + 0 * TOE_CP + t * 16) = u32x4{od[1], od[2], od[3], od[4]};
    *(LAS u32x4*)(toe + 1 * TOE_CP + t * 16) = u32x4{ev_[1], ev_[2], ev_[3], ev_[4]};
    *(LAS u32x4*)(toe + 2 * TOE_CP + t * 16) = u32x4{od[0], od[1], od[2], od[3]};
    *(LAS u32x4*)(toe + 3 * TOE_CP + t * 16) = u32x4{ev_[0], ev_[1], ev_[2], ev_[3]};
    if (t < 12) {
      const int s = t & 3, c = 256 + (t >> 2);
      const u32x2 k03 = *(const LAS u32x2*)kdc;
      const unsigned k0 = k03[0] & 0xffffu, k1 = k03[0] >> 16, k2 = k03[1] & 0xffffu, k3 = k03[1] >> 16;
      unsigned d0 = s == 0 ? k0 : s == 1 ? (k1 | (k0 << 16)) : s == 2 ? (k2 | (k1 << 16)) : (k3 | (k2 << 16));
      unsigned d1 = s == 2 ? k0 : s == 3 ? (k1 | (k0 << 16)) : 0u;
      if (c != 256) { d0 = 0u; d1 = 0u; }
      *(LAS u32x4*)(toe + s * TOE_CP + c * 16) = u32x4{d0, d1, 0u, 0u};
    }
  }
  STAMP(7);
  unsigned ub[4], xc[4];
  {
    for (unsigned spins = 0;; ++spins) {
      bool ok = true;
#pragma unroll
      for (int i = 0; i < 4; ++i) ok = ok && gr[i][0] == tlo_ && gr[i][2] == thi_ && gr[i][1] == gr[i][3];
      if (__all(ok)) break;
      if (spins > kSpinCap) { if (lane == 0) __hip_atomic_store((GAS unsigned*)(ws + WS_TMO), 1u, __ATOMIC_RELAXED, __HIP_MEMORY_SCOPE_AGENT); break; }
      __builtin_amdgcn_s_sleep(1);
#pragma unroll
      for (int i = 0; i < 4; ++i) gr[i] = __builtin_amdgcn_raw_buffer_load_b128(rst, (4 * tid + i) * 16, 0, 16);
    }
    xc[0] = (xq[0] & 0xffffu) | (xq[2] << 16); xc[1] = (xq[0] >> 16) | (xq[2] & 0xffff0000u);
    xc[2] = (xq[1] & 0xffffu) | (xq[3] << 16); xc[3] = (xq[1] >> 16) | (xq[3] & 0xffff0000u);
    float u0[4], u1[4];
#pragma unroll
    for (int i = 0; i < 4; ++i) {
      const float mb = bflo(gr[i][1]), ri = rcp_(bfhi(gr[i][1]));
      const unsigned tt = pkbf(bflo(xc[i]) - mb, bfhi(xc[i]) - mb);
      ub[i] = pkbf(bflo(tt) * ri, bfhi(tt) * ri);
      u0[i] = bflo(ub[i]); u1[i] = bfhi(ub[i]);
    }
    *(LAS u32x2*)(smem + A_U16 + 512 + tid * 8) = u32x2{pkh(u0[0], u0[1]), pkh(u0[2], u0[3])};
    *(LAS u32x2*)(smem + A_U16 + 4608 + 512 + tid * 8) = u32x2{pkh(u1[0], u1[1]), pkh(u1[2], u1[3])};
    if (t < 32) *(LAS u32x4*)(smem + A_U16 + c2 * 4608 + t * 16) = u32x4{0u, 0u, 0u, 0u};
  }
  STAMP(8);
  __syncthreads();
  STAMP(9);
  if (b == 0 && tid == 0) __hip_atomic_store((GAS u64*)(ws + WS_EPOCH), tag, __ATOMIC_RELAXED, __HIP_MEMORY_SCOPE_AGENT);
  {
    const int bq = lane & 15, g = lane >> 4;
    f32x4 acc[8];
#pragma unroll
    for (int i = 0; i < 8; ++i) acc[i] = f32x4{0.f, 0.f, 0.f, 0.f};
    const LAS char* ta = smem + A_TOE2 + c2 * TOE_CH + (bq & 3) * TOE_CP + (2032 - 4 * (bq >> 2) + 8 * g) * 2 - 64 * w4 - 512 * 7 - 256;
    const LAS char* ua = smem + A_U16 + c2 * 4608 + (256 + 16 * (bq - 1) + 8 * g) * 2 - 64 * w4 - 256;
    u32x4 A[2][8], B[2][8];
#pragma unroll
    for (int it = 0; it < 2; ++it) {
#pragma unroll
      for (int k = 0; k < 8; ++k) {
        B[it][k] = *(const LAS u32x4*)(ua + k * 512 + (1 - it) * 256);
        const u32x2 lo = *(const volatile LAS u32x2*)(ta + (7 - k) * 512 + (1 - it) * 256), hi = *(const volatile LAS u32x2*)(ta + (7 - k) * 512 + (1 - it) * 256 + 8);
        A[it][k] = u32x4{lo[0], lo[1], hi[0], hi[1]};
      }
    }
    SB();
#pragma unroll
    for (int it = 0; it < 2; ++it) {
#pragma unroll
      for (int k = 0; k < 8; ++k)
#pragma unroll
        for (int i = k; i < 8; ++i) acc[i] = mfma16h(A[it][k], B[it][i - k], acc[i]);
      SB();
    }
    LAS char* yp = smem + A_YP + w * 8192;
#pragma unroll
    for (int i = 0; i < 8; ++i) *(LAS f32x4*)(yp + i * 1024 + bq * 64 + g * 16) = acc[i];
  }
  STAMP(10);
  __syncthreads();
  STAMP(11);
  {
    f32x4 y0 = {0.f, 0.f, 0.f, 0.f}, y1 = y0;
#pragma unroll
    for (int j = 0; j < 4; ++j) {
      y0 += *(const LAS f32x4*)(smem + A_YP + j * 8192 + tid * 16);
      y1 += *(const LAS f32x4*)(smem + A_YP + (4 + j) * 8192 + tid * 16);
    }
    const float d0 = bflo(r_dsk), d1 = bfhi(r_dsk);
    unsigned hp[4];
#pragma unroll
    for (int i = 0; i < 4; ++i) {
      const unsigned yb = pkbf(y0[i] * SK0, y1[i] * SK1);
      const unsigned sk = pkbf(bflo(ub[i]) * d0, bfhi(ub[i]) * d1);
      const unsigned y2 = pkbf(bflo(yb) + bflo(sk), bfhi(yb) + bfhi(sk));
      hp[i] = pkbf(bflo(xc[i]) + bflo(y2), bfhi(xc[i]) + bfhi(y2));
    }
    const u32x2 h0 = {(hp[0] & 0xffffu) | (hp[1] << 16), (hp[2] & 0xffffu) | (hp[3] << 16)};
    const u32x2 h1 = {(hp[0] >> 16) | (hp[1] & 0xffff0000u), (hp[2] >> 16) | (hp[3] & 0xffff0000u)};
    auto rh = __builtin_amdgcn_make_buffer_rsrc((void*)(ws + WS_HT), 0, L * D * 2, 0x00020000);
    const int off = (tid >> 4) * (D * 128) + (2 * b) * 128 + (tid & 15) * 8;
    __builtin_amdgcn_raw_buffer_store_b64(h0, rh, off, 0, 16);
    __builtin_amdgcn_raw_buffer_store_b64(h1, rh, off + 128, 0, 16);
  }
  STAMP(12);
  STAMP_FLUSH(0);
}

constexpr int WIN = 10;
constexpr int M_HT = 0;
constexpr int M_ST = 57344;
constexpr int M_CS = M_ST + 2048;
constexpr int M_P = M_CS + 1024;
constexpr int M_LDS = M_P + 4 * 64 * 272;

__global__ __launch_bounds__(256, 1) void k_mlp(char* __restrict__ ws, unsigned short* __restrict__ out) {
  __shared__ __attribute__((aligned(16))) char smem_[M_LDS];
  LAS char* smem = (LAS char*)smem_;
  const int tid = threadIdx.x, lane = tid & 63;
  const int w = __builtin_amdgcn_readfirstlane(tid >> 6);
  const int xcd = blockIdx.x & 7, idx = blockIdx.x >> 3;
  const int rt = xcd * 4 + idx / 7, j = idx % 7;
  STAMP_DECL
  STAMP(0);
  const u64 tag = load_epoch(ws);
  auto rH = __builtin_amdgcn_make_buffer_rsrc((void*)(ws + WS_HT), 0, L * D * 2, 0x00020000);
  auto rW1 = __builtin_amdgcn_make_buffer_rsrc((void*)(ws + WS_W1F), 0, D * F * 2, 0x00020000);
  auto rW2 = __builtin_amdgcn_make_buffer_rsrc((void*)(ws + WS_W2F), 0, D * F * 2, 0x00020000);
  auto rH1 = __builtin_amdgcn_make_buffer_rsrc((void*)(ws + WS_H1F), 0, L * F * 2, 0x00020000);
  const int l16 = lane * 16;
  u32x4 hv[14];
#pragma unroll
  for (int p = 0; p < 14; ++p) hv[p] = __builtin_amdgcn_raw_buffer_load_b128(rH, tid * 16, rt * (D * 128) + p * 4096, 0);
  SB();
  float csp[4];
  {
    auto rC = __builtin_amdgcn_make_buffer_rsrc((void*)(ws + WS_CS), 0, 4 * F * 4, 0x00020000);
#pragma unroll
    for (int k = 0; k < 4; ++k) csp[k] = __uint_as_float(__builtin_amdgcn_raw_buffer_load_b32(rC, lane * 4, (k * F + 256 * j + 64 * w) * 4, 0));
  }
  const int ft0 = j * 8 + w * 2;
  u32x4 wa[2][28];
  auto w1load = [&](int i) {
    const int ks = (((i / 7) + w) & 3) * 7 + (i % 7);
    wa[0][i] = __builtin_amdgcn_raw_buffer_load_b128(rW1, l16, (ft0 * 28 + ks) * 1024, 0);
    wa[1][i] = __builtin_amdgcn_raw_buffer_load_b128(rW1, l16, ((ft0 + 1) * 28 + ks) * 1024, 0);
  };
#pragma unroll
  for (int i = 0; i < WIN; ++i) w1load(i);
  SB();
  {
    const int r = tid >> 3, c = tid & 7;
#pragma unroll
    for (int p = 0; p < 14; ++p) { const int row = 32 * p + r; *(LAS u32x4*)(smem + M_HT + row * 128 + ((c ^ (4 * ((row >> 1) & 1))) * 16)) = hv[p]; }
  }
  SB();
  STAMP(1);
  __syncthreads();
  STAMP(2);
  const int hh = lane >> 5, blk = (lane >> 4) & 1, q = (lane & 15) >> 2, p = lane & 3;
  const int sw = (q >> 1) & 1;
  const LAS char* b0 = smem + M_HT + (8 * hh + q) * 128 + ((2 * blk + (p >> 1) + 4 * sw) * 16) + (p & 1) * 8;
  const LAS char* b1 = smem + M_HT + (8 * hh + q) * 128 + ((2 * blk + (p >> 1) + 4 * (1 - sw)) * 16) + (p & 1) * 8;
  f32x16 acc[2][2];
#pragma unroll
  for (int a = 0; a < 2; ++a)
#pragma unroll
    for (int c = 0; c < 2; ++c)
#pragma unroll
      for (int e = 0; e < 16; ++e) acc[a][c][e] = 0.f;
  float s0 = 0.f, s1 = 0.f, q0 = 0.f, q1 = 0.f;
  const unsigned ONES = 0x3f803f80u;
  u32x4 wb[2][28];
  auto w2load = [&](int i) {
    const int n = i >> 2, fsub = i & 3;
    int jj = j + n; jj = jj >= 7 ? jj - 7 : jj;
    const int fs = 16 * jj + 4 * w + fsub;
    wb[0][i] = __builtin_amdgcn_raw_buffer_load_b128(rW2, l16, ((2 * j) * 112 + fs) * 1024, 0);
    wb[1][i] = __builtin_amdgcn_raw_buffer_load_b128(rW2, l16, ((2 * j + 1) * 112 + fs) * 1024, 0);
  };
  u32x4 f0, f1, n0, n1;
  auto bload = [&](int i, u32x4& o0, u32x4& o1) {
    const int go = ((((i / 7) + w) & 3) * 7 + (i % 7)) * 2048;
    o0 = tr_read2(b0 + go, b0 + go + 512);
    o1 = tr_read2(b1 + go, b1 + go + 512);
  };
  bload(0, f0, f1);
#pragma unroll
  for (int i = 0; i < 28; ++i) {
    if (i + 1 < 28) bload(i + 1, n0, n1);
    if (i + WIN < 28) w1load(i + WIN); else if (i + WIN < 36) w2load(i + WIN - 28);
    SB();
    acc[0][0] = mfma32b(wa[0][i], f0, acc[0][0]);
    acc[0][1] = mfma32b(wa[0][i], f1, acc[0][1]);
    acc[1][0] = mfma32b(wa[1][i], f0, acc[1][0]);
    acc[1][1] = mfma32b(wa[1][i], f1, acc[1][1]);
    if (i < 7) {
#pragma unroll
      for (int e = 0; e < 4; ++e) { s0 = dot2bf(f0[e], ONES, s0); q0 = dot2bf(f0[e], f0[e], q0); s1 = dot2bf(f1[e], ONES, s1); q1 = dot2bf(f1[e], f1[e], q1); }
    }
    SB();
    f0 = n0; f1 = n1;
  }
  STAMP(3);
  s0 += __shfl_xor(s0, 32); s1 += __shfl_xor(s1, 32); q0 += __shfl_xor(q0, 32); q1 += __shfl_xor(q1, 32);
  if (hh == 0) {
    *(LAS u32x2*)(smem + M_ST + (w * 64 + lane) * 8) = u32x2{__float_as_uint(s0), __float_as_uint(q0)};
    *(LAS u32x2*)(smem + M_ST + (w * 64 + 32 + lane) * 8) = u32x2{__float_as_uint(s1), __float_as_uint(q1)};
  }
  *(LAS float*)(smem + M_CS + (w * 64 + lane) * 4) = (csp[0] + csp[1]) + (csp[2] + csp[3]);
  __syncthreads();
  STAMP(4);
  float mr[2], ri[2];
#pragma unroll
  for (int c = 0; c < 2; ++c) {
    float s = 0.f, qq = 0.f;
#pragma unroll
    for (int ww = 0; ww < 4; ++ww) { u32x2 v = *(const LAS u32x2*)(smem + M_ST + (ww * 64 + c * 32 + (lane & 31)) * 8); s += __uint_as_float(v[0]); qq += __uint_as_float(v[1]); }
    const float mean = s * (1.0f / D);
    const float var = fmaxf(qq * (1.0f / D) - mean * mean, 0.f);
    float mb, sb; ln_scalars(mean, var, mb, sb);
    mr[c] = mb; ri[c] = rcp_(sb);
  }
  u32x4 own[2][4];
#pragma unroll
  for (int a = 0; a < 2; ++a) {
    float cs[16];
#pragma unroll
    for (int g4 = 0; g4 < 4; ++g4) {
      f32x4 v = *(const LAS f32x4*)(smem + M_CS + (w * 64 + a * 32 + 8 * g4 + 4 * hh) * 4);
#pragma unroll
      for (int e = 0; e < 4; ++e) cs[4 * g4 + e] = v[e];
    }
#pragma unroll
    for (int c = 0; c < 2; ++c) {
#pragma unroll
      for (int s = 0; s < 2; ++s) {
        u32x4 fr;
#pragma unroll
        for (int e = 0; e < 4; ++e) {
          const int r0 = 8 * s + 2 * e;
          const float v0 = (acc[a][c][r0] - mr[c] * cs[r0]) * ri[c], v1 = (acc[a][c][r0 + 1] - mr[c] * cs[r0 + 1]) * ri[c];
          fr[e] = pkbf(fmaxf(v0, 0.f), fmaxf(v1, 0.f));
        }
        own[c][2 * a + s] = fr;
        const int fs = 16 * j + 4 * w + 2 * a + s;
        __builtin_amdgcn_raw_buffer_store_b128(fr, rH1, l16, ((rt * 2 + c) * 112 + fs) * 1024, 16);
      }
    }
  }
  SB();
#pragma unroll
  for (int i = 8; i < 14; ++i) w2load(i);
  SB();
  asm volatile("s_waitcnt vmcnt(12)" ::: "memory");
  GAS u64* flg = (GAS u64*)(ws + WS_FLAGB) + (rt * 4 + w) * 8;
  if (lane == 0) __hip_atomic_store(flg + j, tag, __ATOMIC_RELAXED, __HIP_MEMORY_SCOPE_AGENT);
  SB();
#pragma unroll
  for (int i = 14; i < 28; ++i) w2load(i);
  SB();
  STAMP(5);
#pragma unroll
  for (int a = 0; a < 2; ++a)
#pragma unroll
    for (int c = 0; c < 2; ++c)
#pragma unroll
      for (int e = 0; e < 16; ++e) acc[a][c][e] = 0.f;
#pragma unroll
  for (int i = 0; i < 4; ++i) {
    acc[0][0] = mfma32b(wb[0][i], own[0][i], acc[0][0]);
    acc[0][1] = mfma32b(wb[0][i], own[1][i], acc[0][1]);
    acc[1][0] = mfma32b(wb[1][i], own[0][i], acc[1][0]);
    acc[1][1] = mfma32b(wb[1][i], own[1][i], acc[1][1]);
  }
  SB();
  {
    const int fl = lane < 7 ? lane : 0;
    for (unsigned spins = 0;; ++spins) {
      const u64 v = __hip_atomic_load(flg + fl, __ATOMIC_RELAXED, __HIP_MEMORY_SCOPE_AGENT);
      if (__all(v == tag)) break;
      if (spins > kSpinCap) { if (lane == 0) __hip_atomic_store((GAS unsigned*)(ws + WS_TMO), 2u, __ATOMIC_RELAXED, __HIP_MEMORY_SCOPE_AGENT); break; }
      __builtin_amdgcn_s_sleep(1);
    }
  }
  asm volatile("" ::: "memory");
  STAMP(6);
  u32x4 hf[2][24];
#pragma unroll
  for (int i = 4; i < 28; ++i) {
    const int n = i >> 2, fsub = i & 3;
    int jj = j + n; jj = jj >= 7 ? jj - 7 : jj;
    const int fs = 16 * jj + 4 * w + fsub;
    hf[0][i - 4] = __builtin_amdgcn_raw_buffer_load_b128(rH1, l16, ((rt * 2) * 112 + fs) * 1024, 16);
    hf[1][i - 4] = __builtin_amdgcn_raw_buffer_load_b128(rH1, l16, ((rt * 2 + 1) * 112 + fs) * 1024, 16);
  }
  SB();
#pragma unroll
  for (int i = 4; i < 28; ++i) {
    acc[0][0] = mfma32b(wb[0][i], hf[0][i - 4], acc[0][0]);
    acc[0][1] = mfma32b(wb[0][i], hf[1][i - 4], acc[0][1]);
    acc[1][0] = mfma32b(wb[1][i], hf[0][i - 4], acc[1][0]);
    acc[1][1] = mfma32b(wb[1][i], hf[1][i - 4], acc[1][1]);
    SB();
  }
  STAMP(7);
#pragma unroll
  for (int a = 0; a < 2; ++a)
#pragma unroll
    for (int c = 0; c < 2; ++c)
#pragma unroll
      for (int g4 = 0; g4 < 4; ++g4) {
        f32x4 v = {acc[a][c][4 * g4], acc[a][c][4 * g4 + 1], acc[a][c][4 * g4 + 2], acc[a][c][4 * g4 + 3]};
        *(LAS f32x4*)(smem + M_P + (w * 64 + c * 32 + (lane & 31)) * 272 + (a * 32 + 8 * g4 + 4 * hh) * 4) = v;
      }
  __syncthreads();
  STAMP(8);
  {
    const int l = lane, c4 = w;
    float o[16];
#pragma unroll
    for (int e = 0; e < 16; ++e) o[e] = 0.f;
#pragma unroll
    for (int ww = 0; ww < 4; ++ww)
#pragma unroll
      for (int i = 0; i < 4; ++i) {
        f32x4 v = *(const LAS f32x4*)(smem + M_P + (ww * 64 + l) * 272 + (16 * c4 + 4 * i) * 4);
#pragma unroll
        for (int e = 0; e < 4; ++e) o[4 * i + e] += v[e];
      }
    unsigned pk[8];
#pragma unroll
    for (int e = 0; e < 8; ++e) {
      const int r0 = 64 * j + 16 * c4 + 2 * e, r1 = r0 + 1;
      const float h0 = bf2f(*(const LAS unsigned short*)(smem + M_HT + r0 * 128 + (((l >> 3) ^ (4 * ((r0 >> 1) & 1))) * 16) + (l & 7) * 2));
      const float h1 = bf2f(*(const LAS unsigned short*)(smem + M_HT + r1 * 128 + (((l >> 3) ^ (4 * ((r1 >> 1) & 1))) * 16) + (l & 7) * 2));
      const unsigned ob = pkbf(o[2 * e], o[2 * e + 1]);
      pk[e] = pkbf(h0 + bflo(ob), h1 + bfhi(ob));
    }
    char* op = (char*)out + ((size_t)(rt * 64 + l) * D + 64 * j + 16 * c4) * 2;
    *(u32x4*)op = u32x4{pk[0], pk[1], pk[2], pk[3]};
    *(u32x4*)(op + 16) = u32x4{pk[4], pk[5], pk[6], pk[7]};
  }
  STAMP(9);
  STAMP_FLUSH(1);
}

}

extern "C" void kernel_launch(void* const* d_in, const int* in_sizes, int n_in, void* d_out, int out_size, void* d_ws, size_t ws_size, hipStream_t stream) {
  typedef const unsigned short* cp;
  k_ssm<<<256, 512, 0, stream>>>((cp)d_in[0], (cp)d_in[1], (cp)d_in[2], (cp)d_in[3], (cp)d_in[4], (cp)d_in[5], (cp)d_in[6], (cp)d_in[7], (cp)d_in[8], (cp)d_in[10], (cp)d_in[11], (char*)d_ws);
  k_mlp<<<224, 256, 0, stream>>>((char*)d_ws, (unsigned short*)d_out);
}
```

```cpp
#include <hip/hip_runtime.h>
#include <stdint.h>

namespace {

typedef __attribute__((ext_vector_type(8))) _Float16 f16x8;
typedef __attribute__((ext_vector_type(4))) _Float16 f16x4;
typedef __attribute__((ext_vector_type(2))) _Float16 f16x2;
typedef __attribute__((ext_vector_type(8))) __bf16 bf16x8;
typedef __attribute__((ext_vector_type(2))) __bf16 bf16x2;
typedef __attribute__((ext_vector_type(4))) float f32x4;
typedef __attribute__((ext_vector_type(16))) float f32x16;
typedef __attribute__((ext_vector_type(4))) unsigned u32x4;
typedef __attribute__((ext_vector_type(2))) unsigned u32x2;
typedef __attribute__((ext_vector_type(4))) short s16x4;
typedef unsigned long long u64;
#define LAS __attribute__((address_space(3)))
#define GAS __attribute__((address_space(1)))
#define DI __device__ __forceinline__

constexpr int L = 2048, D = 448, NM = 256, F = 1792;

constexpr size_t WS_EPOCH = 0;
constexpr size_t WS_TMO = 64;
constexpr size_t WS_FLAGB = 8192;
constexpr size_t WS_STATS = 16384;
constexpr size_t WS_XT = WS_STATS + 2048 * 16;
constexpr size_t WS_CS = WS_XT + (size_t)512 * D * 16;
constexpr size_t WS_HT = WS_CS + 4 * F * 4;
constexpr size_t WS_W1F = WS_HT + (size_t)D * L * 2;
constexpr size_t WS_W2F = WS_W1F + (size_t)D * F * 2;
constexpr size_t WS_H1F = WS_W2F + (size_t)D * F * 2;
constexpr size_t WS_END = WS_H1F + (size_t)L * F * 2;
constexpr size_t WS_STAMP = WS_END;
constexpr unsigned kSpinCap = 1u << 20;

#define STAMP(i) do {} while (0)
#define STAMP_DECL
#define STAMP_FLUSH(k) do {} while (0)
#define SB() __builtin_amdgcn_sched_barrier(0)
DI float bf2f(unsigned b) { return __uint_as_float(b << 16); }
DI float bflo(unsigned p) { return __uint_as_float(p << 16); }
DI float bfhi(unsigned p) { return __uint_as_float(p & 0xffff0000u); }
DI unsigned pkbf(float a, float b) { bf16x2 r; r[0] = (__bf16)a; r[1] = (__bf16)b; return __builtin_bit_cast(unsigned, r); }
DI float rbf(float a) { return bflo(pkbf(a, a)); }
DI unsigned pkh(float a, float b) { f16x2 r; r[0] = (_Float16)a; r[1] = (_Float16)b; return __builtin_bit_cast(unsigned, r); }

template <int CTRL> DI float dppf(float v) { return __int_as_float(__builtin_amdgcn_update_dpp(0, __float_as_int(v), CTRL, 0xF, 0xF, true)); }
DI float rdl(float v, int l) { return __int_as_float(__builtin_amdgcn_readlane(__float_as_int(v), l)); }
DI float wave_sum(float v) {
  v += dppf<0xB1>(v); v += dppf<0x4E>(v); v += dppf<0x141>(v); v += dppf<0x140>(v);
  return (rdl(v, 0) + rdl(v, 16)) + (rdl(v, 32) + rdl(v, 48));
}
DI float wave_max(float v) {
  v = fmaxf(v, dppf<0xB1>(v)); v = fmaxf(v, dppf<0x4E>(v)); v = fmaxf(v, dppf<0x141>(v)); v = fmaxf(v, dppf<0x140>(v));
  return fmaxf(fmaxf(rdl(v, 0), rdl(v, 16)), fmaxf(rdl(v, 32), rdl(v, 48)));
}
DI s16x4 tr_read(const LAS char* p) { return __builtin_amdgcn_ds_read_tr16_b64_v4i16((LAS s16x4*)p); }
DI u32x4 tr_read2(const LAS char* p0, const LAS char* p1) {
  s16x4 a = tr_read(p0), b = tr_read(p1);
  u32x2 a2 = __builtin_bit_cast(u32x2, a), b2 = __builtin_bit_cast(u32x2, b);
  u32x4 r; r[0] = a2[0]; r[1] = a2[1]; r[2] = b2[0]; r[3] = b2[1]; return r;
}
DI f32x4 mfma16h(u32x4 a, u32x4 b, f32x4 c) { return __builtin_amdgcn_mfma_f32_16x16x32_f16(__builtin_bit_cast(f16x8, a), __builtin_bit_cast(f16x8, b), c, 0, 0, 0); }
DI f32x16 mfma32b(u32x4 a, u32x4 b, f32x16 c) { return __builtin_amdgcn_mfma_f32_32x32x16_bf16(__builtin_bit_cast(bf16x8, a), __builtin_bit_cast(bf16x8, b), c, 0, 0, 0); }
DI float dot2bf(unsigned a, unsigned b, float c) { return __builtin_amdgcn_fdot2_f32_bf16(__builtin_bit_cast(bf16x2, a), __builtin_bit_cast(bf16x2, b), c, false); }

DI u64 load_epoch(const char* ws) {
  const u32x2 v = *(const u32x2*)(ws + WS_EPOCH);
  unsigned lo = __builtin_amdgcn_readfirstlane(v[0]), hi = __builtin_amdgcn_readfirstlane(v[1]);
  asm volatile("" : "+s"(lo), "+s"(hi));
  return ((u64)hi << 32) | lo;
}
struct cf { float r, i; };
DI cf cmul(cf a, cf b) { cf o; o.r = a.r * b.r - a.i * b.i; o.i = a.r * b.i + a.i * b.r; return o; }
DI cf csq(cf a) { cf o; o.r = a.r * a.r - a.i * a.i; o.i = 2.f * a.r * a.i; return o; }
DI cf cexp_p2(float zr2, float zf, float p2) {
  float m = __builtin_amdgcn_exp2f(zr2 * p2);
  float t = __builtin_amdgcn_fractf(zf * p2);
  cf o; o.r = m * __builtin_amdgcn_cosf(t); o.i = m * __builtin_amdgcn_sinf(t); return o;
}

DI void ln_scalars(float mean, float var, float& mb, float& sb) {
  mb = rbf(mean);
  float vb = rbf(var);
  sb = rbf(__builtin_sqrtf(rbf(vb + 1e-5f)));
}
DI float rcp_(float s) { return __builtin_amdgcn_rcpf(s); }

struct IdxTab { unsigned short v[256][12]; };
constexpr int bf16_round_index(int lag) {
  if (lag < 0 || lag > 2047) return 641;
  if (lag < 256) return lag;
  int e = 8; while ((lag >> (e + 1)) != 0) ++e;
  const int sh = e - 7;
  int q = lag >> sh; const int r = lag & ((1 << sh) - 1), half = 1 << (sh - 1);
  if (r > half || (r == half && (q & 1))) ++q;
  return 256 + (e - 8) * 128 + (q - 128);
}
constexpr IdxTab make_idx() {
  IdxTab t{};
  for (int c = 0; c < 256; ++c) { for (int e = 0; e < 11; ++e) t.v[c][e] = (unsigned short)(2 * bf16_round_index(2048 - 8 * c + 3 - e)); t.v[c][11] = 0; }
  return t;
}
__device__ const IdxTab g_idx = make_idx();

constexpr int A_TAB = 0;
constexpr int A_MISC = A_TAB + 8 * 16384;
constexpr int A_KDC = A_MISC;
constexpr int A_SC = A_KDC + 2 * 1312;
constexpr int A_XS = A_SC + 128;
constexpr int A_LDS = A_XS + 8192;
constexpr int TOE_CP = 4160, TOE_CH = 4 * TOE_CP;
constexpr int A_U16 = A_TAB;
constexpr int A_TOE2 = A_TAB + 9216;
constexpr int A_YP = A_TAB + 42496;

__global__ __launch_bounds__(512, 2) void k_ssm(const unsigned short* __restrict__ x, const unsigned short* __restrict__ Are, const unsigned short* __restrict__ Aim,
                                                 const unsigned short* __restrict__ Bre, const unsigned short* __restrict__ Bim, const unsigned short* __restrict__ Cre,
                                                 const unsigned short* __restrict__ Cim, const unsigned short* __restrict__ logdt, const unsigned short* __restrict__ Dsk,
                                                 const unsigned short* __restrict__ w1, const unsigned short* __restrict__ w2, char* __restrict__ ws) {
  __shared__ __attribute__((aligned(16))) char smem_[A_LDS];
  LAS char* smem = (LAS char*)smem_;
  const int tid = threadIdx.x, lane = tid & 63;
  const int w = __builtin_amdgcn_readfirstlane(tid >> 6);
  const int b = blockIdx.x;
  const bool has_ch = b < 224;
  const int c2 = w >> 2, w4 = w & 3;
  const int ch = 2 * b + c2;
  STAMP_DECL
  STAMP(0);
  const int bs = has_ch ? b : 0;
  const int chs = 2 * bs + c2;
  const int po = chs * NM + w4 * 64 + lane;
  const unsigned short r_are = Are[po], r_aim = Aim[po], r_bre = Bre[po], r_bim = Bim[po], r_cre = Cre[po], r_cim = Cim[po];
  const unsigned short r_ldt = logdt[chs];
  const u32x2 ev = *(const u32x2*)(ws + WS_EPOCH);
  SB();
  const int row = 8 * b + w;
  const int trt = bs & 31, tg = bs >> 5;
  const u32x4 xt = *(const u32x4*)(x + (size_t)(64 * trt + 8 * w + (lane >> 3)) * D + 64 * tg + (lane & 7) * 8);
  u32x4 xv = *(const u32x4*)(x + (size_t)row * D + min(lane, 55) * 8);
  const unsigned r_dsk = *(const unsigned*)(Dsk + 2 * bs);
  const int t = tid & 255;
  const u32x4 ix0 = *(const u32x4*)&g_idx.v[t][0];
  const u32x2 ix1 = *(const u32x2*)&g_idx.v[t][8];
  SB();
  unsigned elo = __builtin_amdgcn_readfirstlane(ev[0]), ehi = __builtin_amdgcn_readfirstlane(ev[1]);
  asm volatile("" : "+s"(elo), "+s"(ehi));
  const u64 tag = (((u64)ehi << 32) | elo) + 1;
  const unsigned tlo_ = (unsigned)tag, thi_ = (unsigned)(tag >> 32);
  LAS char* tab = smem + A_TAB + w * 16384;

  auto ln_stats = [&]() {
    if (lane >= 56) xv = u32x4{0u, 0u, 0u, 0u};
    float xf[8];
#pragma unroll
    for (int j = 0; j < 4; ++j) { xf[2 * j] = bflo(xv[j]); xf[2 * j + 1] = bfhi(xv[j]); }
    float s = 0.f;
#pragma unroll
    for (int j = 0; j < 8; ++j) s += xf[j];
    const float mean = wave_sum(s) * (1.0f / D);
    float q = 0.f;
#pragma unroll
    for (int j = 0; j < 8; ++j) { float d = xf[j] - mean; q += d * d; }
    if (lane >= 56) q = 0.f;
    const float var = wave_sum(q) * (1.0f / D);
    float mb, sb; ln_scalars(mean, var, mb, sb);
    const unsigned pay = (__float_as_uint(mb) >> 16) | (__float_as_uint(sb) & 0xffff0000u);
    auto rs = __builtin_amdgcn_make_buffer_rsrc((void*)(ws + WS_STATS), 0, 2048 * 16, 0x00020000);
    if (lane == 0) __builtin_amdgcn_raw_buffer_store_b128(u32x4{tlo_, pay, thi_, pay}, rs, row * 16, 0, 16);
  };
  auto xt_publish = [&]() {
    LAS char* sc = smem + A_XS + w * 1024;
    *(LAS u32x4*)(sc + lane * 16) = xt;
    __builtin_amdgcn_s_waitcnt(0xC07F);
    __builtin_amdgcn_wave_barrier();
    const int g = lane >> 4, q = (lane & 15) >> 2, p = lane & 3;
    const LAS char* a0 = sc + q * 128 + (16 * g + 4 * p) * 2;
    const u32x2 lo = __builtin_bit_cast(u32x2, tr_read(a0)), hi = __builtin_bit_cast(u32x2, tr_read(a0 + 512));
    auto rx = __builtin_amdgcn_make_buffer_rsrc((void*)(ws + WS_XT), 0, 512 * D * 16, 0x00020000);
    const int o = ((trt * 16 + 2 * w) * D + 64 * tg + lane) * 16;
    __builtin_amdgcn_raw_buffer_store_b128(u32x4{lo[0], lo[1], tlo_, thi_}, rx, o, 0, 16);
    __builtin_amdgcn_raw_buffer_store_b128(u32x4{hi[0], hi[1], tlo_, thi_}, rx, o + D * 16, 0, 16);
  };
  STAMP(1);

  if (!has_ch) {
    ln_stats();
    const int task = (b - 224) * 8 + w;
    if (task < 224) {
      const bool is_w1 = task < 112;
      const int tk = is_w1 ? task : task - 112;
      const int nb = is_w1 ? tk % 28 : tk % 7, kg = is_w1 ? tk / 28 : tk / 7;
      const int rld = is_w1 ? F : D;
      const unsigned short* src = (is_w1 ? w1 : w2) + (size_t)(16 * 7 * kg + (lane >> 3)) * rld + 64 * nb + (lane & 7) * 8;
      u32x4 r0[7], r1[7];
#pragma unroll
      for (int u = 0; u < 7; ++u) { r0[u] = *(const u32x4*)(src + (size_t)(16 * u) * rld); r1[u] = *(const u32x4*)(src + (size_t)(16 * u + 8) * rld); }
      LAS char* sc = smem + w * 16384;
#pragma unroll
      for (int u = 0; u < 7; ++u) { *(LAS u32x4*)(sc + u * 2048 + lane * 16) = r0[u]; *(LAS u32x4*)(sc + u * 2048 + 1024 + lane * 16) = r1[u]; }
      __builtin_amdgcn_s_waitcnt(0xC07F);
      __builtin_amdgcn_wave_barrier();
      const int g = lane >> 4, blk = g & 1, hh = g >> 1, q = (lane & 15) >> 2, p = lane & 3;
      const int q0 = is_w1 ? (8 * hh + q) : (4 * hh + q);
      const int q1 = is_w1 ? (8 * hh + 4 + q) : (8 + 4 * hh + q);
      auto rd = __builtin_amdgcn_make_buffer_rsrc((void*)(ws + (is_w1 ? WS_W1F : WS_W2F)), 0, D * F * 2, 0x00020000);
      const int nks = is_w1 ? 28 : 112;
      float cs[2] = {0.f, 0.f};
#pragma unroll
      for (int u = 0; u < 7; ++u) {
#pragma unroll
        for (int tt = 0; tt < 2; ++tt) {
          const int cb = (tt * 32 + blk * 16 + 4 * p) * 2;
          u32x4 fr = tr_read2(sc + u * 2048 + q0 * 128 + cb, sc + u * 2048 + q1 * 128 + cb);
          __builtin_amdgcn_raw_buffer_store_b128(fr, rd, lane * 16, ((2 * nb + tt) * nks + 7 * kg + u) * 1024, 16);
#pragma unroll
          for (int e = 0; e < 4; ++e) cs[tt] = dot2bf(fr[e], 0x3f803f80u, cs[tt]);
        }
      }
      if (is_w1) {
        cs[0] += __shfl_xor(cs[0], 32); cs[1] += __shfl_xor(cs[1], 32);
        auto rc = __builtin_amdgcn_make_buffer_rsrc((void*)(ws + WS_CS), 0, 4 * F * 4, 0x00020000);
        __builtin_amdgcn_raw_buffer_store_b32(__float_as_uint(lane < 32 ? cs[0] : cs[1]), rc, (kg * F + 64 * nb + lane) * 4, 0, 16);
      }
    }
    return;
  }

  STAMP(13);
  float k2048;
  int ew;
  {
    const float are = bf2f(r_are), aim = bf2f(r_aim), bre = bf2f(r_bre), bim = bf2f(r_bim), cre = bf2f(r_cre), cim = bf2f(r_cim);
    const float dt = rbf(expf(bf2f(r_ldt)));
    const float zr = dt * are, zi = dt * aim;
    const float zr2 = zr * 1.44269504088896341f, zf = zi * 0.15915494309189535f;
    const float dbr = dt * bre, dbi = dt * bim;
    cf CB; CB.r = cre * dbr - cim * dbi; CB.i = cre * dbi + cim * dbr;
    const float mx = wave_max(fmaxf(fabsf(CB.r), fabsf(CB.i)));
    const float gz = fmaxf(wave_max(zr2), 0.f) * 2048.f;
    int e = (mx > 0.f) ? ((int)(__float_as_uint(mx) >> 23) - 127) : 0;
    e += (int)ceilf(fminf(gz, 100.f));
    e = max(-120, min(120, e));
    ew = e;
    const float S = __uint_as_float((unsigned)(127 - e) << 23);
    cf G0; G0.r = CB.r * S; G0.i = CB.i * S;
    const cf E1 = cexp_p2(zr2, zf, 1.f), E2 = csq(E1), E4 = cexp_p2(zr2, zf, 4.f), E8 = csq(E4);
    const cf E16 = cexp_p2(zr2, zf, 16.f), E32 = csq(E16), E64 = cexp_p2(zr2, zf, 64.f), E128 = csq(E64);
    const cf E256 = cexp_p2(zr2, zf, 256.f), E512 = csq(E256), E1024 = cexp_p2(zr2, zf, 1024.f), E2048 = csq(E1024);
    STAMP(14);
    SB();
    xt_publish();
    ln_stats();
    SB();
    const int sw = (lane >> 1) & 3;
    LAS char* tb = tab + lane * 64;
    auto put = [&](int tile, int half, const cf* v, float isign) {
      u32x4 re, im;
#pragma unroll
      for (int j = 0; j < 4; ++j) { re[j] = pkh(v[2 * j].r, v[2 * j + 1].r); im[j] = pkh(isign * v[2 * j].i, isign * v[2 * j + 1].i); }
      *(LAS u32x4*)(tb + tile * 4096 + ((half ^ sw) * 16)) = re;
      *(LAS u32x4*)(tb + tile * 4096 + (((2 + half) ^ sw) * 16)) = im;
    };
    cf Er[8];
    Er[0].r = 1.f; Er[0].i = 0.f; Er[1] = E1; Er[2] = E2; Er[3] = cmul(E1, E2); Er[4] = E4; Er[5] = cmul(E4, E1); Er[6] = cmul(E4, E2); Er[7] = cmul(E4, Er[3]);
    {
      cf Y[8];
      Y[0].r = 1.f; Y[0].i = 0.f; Y[1] = E8; Y[2] = E16; Y[3] = cmul(E8, E16); Y[4] = E32; Y[5] = cmul(E32, E8); Y[6] = cmul(E32, E16); Y[7] = cmul(E32, Y[3]);
      put(3, 0, Y, 1.f);
      cf Y2[8];
      Y2[0] = E64;
#pragma unroll
      for (int j = 1; j < 8; ++j) Y2[j] = cmul(Y[j], E64);
      put(3, 1, Y2, 1.f);
    }
    STAMP(15);
    cf G[8];
    G[0] = G0; G[1] = cmul(G0, E128); G[2] = cmul(G0, E256); G[3] = cmul(G[1], E256);
#pragma unroll
    for (int j = 0; j < 4; ++j) G[4 + j] = cmul(G[j], E512);
    {
      cf T[8];
      T[0] = G[0];
#pragma unroll
      for (int j = 1; j < 8; ++j) T[j] = cmul(G[0], Er[j]);
      put(0, 0, T, -1.f);
      T[0] = G[1];
#pragma unroll
      for (int j = 1; j < 8; ++j) T[j] = cmul(G[1], Er[j]);
      put(0, 1, T, -1.f);
    }
    {
      cf T[8];
      T[0] = G[2]; T[1] = cmul(G[2], E2); T[2] = cmul(G[2], E4); T[3] = cmul(G[2], Er[6]);
      T[4] = G[3]; T[5] = cmul(G[3], E2); T[6] = cmul(G[3], E4); T[7] = cmul(G[3], Er[6]);
      put(1, 0, T, -1.f);
#pragma unroll
      for (int j = 0; j < 4; ++j) { T[2 * j] = G[4 + j]; T[2 * j + 1] = cmul(G[4 + j], E4); }
      put(1, 1, T, -1.f);
    }
    {
      cf T[8];
#pragma unroll
      for (int j = 0; j < 8; ++j) T[j] = cmul(G[j], E1024);
      put(2, 0, T, -1.f);
    }
    k2048 = wave_sum(G0.r * E2048.r - G0.i * E2048.i);
  }
  STAMP(2);
  __builtin_amdgcn_s_waitcnt(0xC07F);
  __builtin_amdgcn_wave_barrier();
  {
    const int g = lane >> 4, q = (lane & 15) >> 2, p = lane & 3;
    f32x4 acc0 = {0.f, 0.f, 0.f, 0.f}, acc1 = acc0, acc2 = acc0;
    const int part = q & 1, cl = part * 2 + (p >> 1);
    u32x4 bb[4], a0[4], a1[4], a2[4];
#pragma unroll
    for (int s = 0; s < 4; ++s) {
      const int m0 = 16 * s + 8 * (g >> 1) + 2 * (g & 1) + (q >> 1), m1 = m0 + 4;
      const int o0 = m0 * 64 + ((cl ^ ((m0 >> 1) & 3)) * 16) + (p & 1) * 8;
      const int o1 = m1 * 64 + ((cl ^ ((m1 >> 1) & 3)) * 16) + (p & 1) * 8;
      bb[s] = tr_read2(tab + 3 * 4096 + o0, tab + 3 * 4096 + o1);
      a0[s] = tr_read2(tab + o0, tab + o1);
      a1[s] = tr_read2(tab + 4096 + o0, tab + 4096 + o1);
      a2[s] = tr_read2(tab + 8192 + o0, tab + 8192 + o1);
    }
    SB();
#pragma unroll
    for (int s = 0; s < 4; ++s) { acc0 = mfma16h(a0[s], bb[s], acc0); acc1 = mfma16h(a1[s], bb[s], acc1); acc2 = mfma16h(a2[s], bb[s], acc2); }
    SB();
    *(LAS f32x4*)(tab + lane * 16) = acc0;
    *(LAS f32x4*)(tab + 1024 + lane * 16) = acc1;
    *(LAS f32x4*)(tab + 2048 + lane * 16) = acc2;
    if (lane == 0) { *(LAS int*)(smem + A_SC + w * 8) = ew; *(LAS float*)(smem + A_SC + w * 8 + 4) = k2048; }
  }
  STAMP(3);
  auto rst = __builtin_amdgcn_make_buffer_rsrc((void*)(ws + WS_STATS), 0, 2048 * 16, 0x00020000);
  u32x4 gr[4];
#pragma unroll
  for (int i = 0; i < 4; ++i) gr[i] = __builtin_amdgcn_raw_buffer_load_b128(rst, (4 * tid + i) * 16, 0, 16);
  auto rxq = __builtin_amdgcn_make_buffer_rsrc((void*)(ws + WS_XT), 0, 512 * D * 16, 0x00020000);
  u32x4 xq0 = __builtin_amdgcn_raw_buffer_load_b128(rxq, (tid * D + 2 * b) * 16, 0, 16);
  u32x4 xq1 = __builtin_amdgcn_raw_buffer_load_b128(rxq, (tid * D + 2 * b) * 16 + 16, 0, 16);
  __syncthreads();
  STAMP(4);
  float SKinv;
  {
    int e4[4]; float k4[4];
#pragma unroll
    for (int j = 0; j < 4; ++j) { e4[j] = *(LAS int*)(smem + A_SC + (c2 * 4 + j) * 8); k4[j] = *(LAS float*)(smem + A_SC + (c2 * 4 + j) * 8 + 4); }
    const int em = max(max(e4[0], e4[1]), max(e4[2], e4[3]));
    float fw[4];
#pragma unroll
    for (int j = 0; j < 4; ++j) { int d = max(e4[j] - em + 5, -120); fw[j] = __uint_as_float((unsigned)(127 + d) << 23); }
    SKinv = __uint_as_float((unsigned)(127 + em - 5) << 23);
    LAS char* kdc = smem + A_KDC + c2 * 1312;
    if (t < 192) {
      const int tile = t >> 6, ln = t & 63, c = ln & 15, g = ln >> 4;
      f32x4 s = {0.f, 0.f, 0.f, 0.f};
#pragma unroll
      for (int j = 0; j < 4; ++j) { f32x4 v = *(LAS f32x4*)(smem + A_TAB + (c2 * 4 + j) * 16384 + tile * 1024 + ln * 16); s += v * fw[j]; }
      const unsigned p01 = pkh(s[0], s[1]), p23 = pkh(s[2], s[3]);
      if (tile == 0) {
        u32x2 v = {p01, p23}; *(LAS u32x2*)(kdc + (128 * (g >> 1) + 8 * c + 4 * (g & 1)) * 2) = v;
      } else if (tile == 1) {
        if (g < 2) { u32x2 v = {p01, p23}; *(LAS u32x2*)(kdc + (256 + 64 * g + 4 * c) * 2) = v; }
        else { const int i0 = 384 + 64 * (g - 2) + 2 * c; *(LAS unsigned*)(kdc + i0 * 2) = p01; *(LAS unsigned*)(kdc + (i0 + 32) * 2) = p23; }
      } else if (g < 2) {
        const int i0 = 512 + 64 * g + c;
        *(LAS unsigned short*)(kdc + i0 * 2) = (unsigned short)p01; *(LAS unsigned short*)(kdc + (i0 + 16) * 2) = (unsigned short)(p01 >> 16);
        *(LAS unsigned short*)(kdc + (i0 + 32) * 2) = (unsigned short)p23; *(LAS unsigned short*)(kdc + (i0 + 48) * 2) = (unsigned short)(p23 >> 16);
      }
    } else if (t == 192) {
      const float k = k4[0] * fw[0] + k4[1] * fw[1] + k4[2] * fw[2] + k4[3] * fw[3];
      *(LAS unsigned*)(kdc + 640 * 2) = pkh(k, 0.f);
    }
  }
  float SKo;
  {
    int e4[4];
#pragma unroll
    for (int j = 0; j < 4; ++j) e4[j] = *(LAS int*)(smem + A_SC + ((1 - c2) * 4 + j) * 8);
    const int em = max(max(e4[0], e4[1]), max(e4[2], e4[3]));
    SKo = __uint_as_float((unsigned)(127 + em - 5) << 23);
  }
  const float SK0 = c2 ? SKo : SKinv, SK1 = c2 ? SKinv : SKo;
  STAMP(5);
  __syncthreads();
  STAMP(6);
  {
    const LAS char* kdc = smem + A_KDC + c2 * 1312;
    LAS char* toe = smem + A_TOE2 + c2 * TOE_CH;
    unsigned v[11];
#pragma unroll
    for (int e = 0; e < 11; ++e) {
      const unsigned wd = e < 8 ? ix0[e >> 1] : ix1[(e - 8) >> 1];
      const unsigned off = (e & 1) ? (wd >> 16) : (wd & 0xffffu);
      v[e] = *(const LAS unsigned short*)(kdc + off);
    }
    unsigned ev_[5], od[5];
#pragma unroll
    for (int j = 0; j < 5; ++j) { ev_[j] = v[2 * j] | (v[2 * j + 1] << 16); od[j] = v[2 * j + 1] | (v[2 * j + 2] << 16); }
    *(LAS u32x4*)(toe + 0 * TOE_CP + t * 16) = u32x4{od[1], od[2], od[3], od[4]};
    *(LAS u32x4*)(toe + 1 * TOE_CP + t * 16) = u32x4{ev_[1], ev_[2], ev_[3], ev_[4]};
    *(LAS u32x4*)(toe + 2 * TOE_CP + t * 16) = u32x4{od[0], od[1], od[2], od[3]};
    *(LAS u32x4*)(toe + 3 * TOE_CP + t * 16) = u32x4{ev_[0], ev_[1], ev_[2], ev_[3]};
    if (t < 12) {
      const int s = t & 3, c = 256 + (t >> 2);
      const u32x2 k03 = *(const LAS u32x2*)kdc;
      const unsigned k0 = k03[0] & 0xffffu, k1 = k03[0] >> 16, k2 = k03[1] & 0xffffu, k3 = k03[1] >> 16;
      unsigned d0 = s == 0 ? k0 : s == 1 ? (k1 | (k0 << 16)) : s == 2 ? (k2 | (k1 << 16)) : (k3 | (k2 << 16));
      unsigned d1 = s == 2 ? k0 : s == 3 ? (k1 | (k0 << 16)) : 0u;
      if (c != 256) { d0 = 0u; d1 = 0u; }
      *(LAS u32x4*)(toe + s * TOE_CP + c * 16) = u32x4{d0, d1, 0u, 0u};
    }
  }
  STAMP(7);
  unsigned ub[4], xc[4];
  {
    for (unsigned spins = 0;; ++spins) {
      bool ok = true;
#pragma unroll
      for (int i = 0; i < 4; ++i) ok = ok && gr[i][0] == tlo_ && gr[i][2] == thi_ && gr[i][1] == gr[i][3];
      ok = ok && xq0[2] == tlo_ && xq0[3] == thi_ && xq1[2] == tlo_ && xq1[3] == thi_;
      if (__all(ok)) break;
      if (spins > kSpinCap) { if (lane == 0) __hip_atomic_store((GAS unsigned*)(ws + WS_TMO), 1u, __ATOMIC_RELAXED, __HIP_MEMORY_SCOPE_AGENT); break; }
      __builtin_amdgcn_s_sleep(1);
#pragma unroll
      for (int i = 0; i < 4; ++i) gr[i] = __builtin_amdgcn_raw_buffer_load_b128(rst, (4 * tid + i) * 16, 0, 16);
      xq0 = __builtin_amdgcn_raw_buffer_load_b128(rxq, (tid * D + 2 * b) * 16, 0, 16);
      xq1 = __builtin_amdgcn_raw_buffer_load_b128(rxq, (tid * D + 2 * b) * 16 + 16, 0, 16);
    }
    xc[0] = (xq0[0] & 0xffffu) | (xq1[0] << 16); xc[1] = (xq0[0] >> 16) | (xq1[0] & 0xffff0000u);
    xc[2] = (xq0[1] & 0xffffu) | (xq1[1] << 16); xc[3] = (xq0[1] >> 16) | (xq1[1] & 0xffff0000u);
    float u0[4], u1[4];
#pragma unroll
    for (int i = 0; i < 4; ++i) {
      const float mb = bflo(gr[i][1]), ri = rcp_(bfhi(gr[i][1]));
      const unsigned tt = pkbf(bflo(xc[i]) - mb, bfhi(xc[i]) - mb);
      ub[i] = pkbf(bflo(tt) * ri, bfhi(tt) * ri);
      u0[i] = bflo(ub[i]); u1[i] = bfhi(ub[i]);
    }
    *(LAS u32x2*)(smem + A_U16 + 512 + tid * 8) = u32x2{pkh(u0[0], u0[1]), pkh(u0[2], u0[3])};
    *(LAS u32x2*)(smem + A_U16 + 4608 + 512 + tid * 8) = u32x2{pkh(u1[0], u1[1]), pkh(u1[2], u1[3])};
    if (t < 32) *(LAS u32x4*)(smem + A_U16 + c2 * 4608 + t * 16) = u32x4{0u, 0u, 0u, 0u};
  }
  STAMP(8);
  __syncthreads();
  STAMP(9);
  if (b == 0 && tid == 0) __hip_atomic_store((GAS u64*)(ws + WS_EPOCH), tag, __ATOMIC_RELAXED, __HIP_MEMORY_SCOPE_AGENT);
  {
    const int bq = lane & 15, g = lane >> 4;
    f32x4 acc[8];
#pragma unroll
    for (int i = 0; i < 8; ++i) acc[i] = f32x4{0.f, 0.f, 0.f, 0.f};
    const LAS char* ta = smem + A_TOE2 + c2 * TOE_CH + (bq & 3) * TOE_CP + (2032 - 4 * (bq >> 2) + 8 * g) * 2 - 64 * w4 - 512 * 7 - 256;
    const LAS char* ua = smem + A_U16 + c2 * 4608 + (256 + 16 * (bq - 1) + 8 * g) * 2 - 64 * w4 - 256;
    u32x4 A[2][8], B[2][8];
    auto rdA = [&](int it, int k) {
      const u32x2 lo = *(const volatile LAS u32x2*)(ta + (7 - k) * 512 + (1 - it) * 256), hi = *(const volatile LAS u32x2*)(ta + (7 - k) * 512 + (1 - it) * 256 + 8);
      A[it][k] = u32x4{lo[0], lo[1], hi[0], hi[1]};
    };
    auto rdB = [&](int it, int k) { B[it][k] = *(const LAS u32x4*)(ua + k * 512 + (1 - it) * 256); };
    rdA(0, 0);
#pragma unroll
    for (int k = 0; k < 8; ++k) rdB(0, k);
    rdA(0, 1);
    SB();
#pragma unroll
    for (int it = 0; it < 2; ++it) {
#pragma unroll
      for (int k = 0; k < 8; ++k) {
        if (k + 2 < 8) rdA(it, k + 2); else if (it == 0) rdA(1, k + 2 - 8);
        if (it == 0) rdB(1, k);
        SB();
#pragma unroll
        for (int i = k; i < 8; ++i) acc[i] = mfma16h(A[it][k], B[it][i - k], acc[i]);
        SB();
      }
    }
    LAS char* yp = smem + A_YP + w * 8192;
#pragma unroll
    for (int i = 0; i < 8; ++i) *(LAS f32x4*)(yp + i * 1024 + bq * 64 + g * 16) = acc[i];
  }
  STAMP(10);
  __syncthreads();
  STAMP(11);
  {
    f32x4 y0 = {0.f, 0.f, 0.f, 0.f}, y1 = y0;
#pragma unroll
    for (int j = 0; j < 4; ++j) {
      y0 += *(const LAS f32x4*)(smem + A_YP + j * 8192 + tid * 16);
      y1 += *(const LAS f32x4*)(smem + A_YP + (4 + j) * 8192 + tid * 16);
    }
    const float d0 = bflo(r_dsk), d1 = bfhi(r_dsk);
    unsigned hp[4];
#pragma unroll
    for (int i = 0; i < 4; ++i) {
      const unsigned yb = pkbf(y0[i] * SK0, y1[i] * SK1);
      const unsigned sk = pkbf(bflo(ub[i]) * d0, bfhi(ub[i]) * d1);
      const unsigned y2 = pkbf(bflo(yb) + bflo(sk), bfhi(yb) + bfhi(sk));
      hp[i] = pkbf(bflo(xc[i]) + bflo(y2), bfhi(xc[i]) + bfhi(y2));
    }
    const u32x2 h0 = {(hp[0] & 0xffffu) | (hp[1] << 16), (hp[2] & 0xffffu) | (hp[3] << 16)};
    const u32x2 h1 = {(hp[0] >> 16) | (hp[1] & 0xffff0000u), (hp[2] >> 16) | (hp[3] & 0xffff0000u)};
    auto rh = __builtin_amdgcn_make_buffer_rsrc((void*)(ws + WS_HT), 0, L * D * 2, 0x00020000);
    const int off = (tid >> 4) * (D * 128) + (2 * b) * 128 + (tid & 15) * 8;
    __builtin_amdgcn_raw_buffer_store_b64(h0, rh, off, 0, 16);
    __builtin_amdgcn_raw_buffer_store_b64(h1, rh, off + 128, 0, 16);
  }
  STAMP(12);
  STAMP_FLUSH(0);
}

constexpr int WIN0 = 8, WIN = 18;
constexpr int M_HT = 0;
constexpr int M_ST = 57344;
constexpr int M_CS = M_ST + 2048;
constexpr int M_P = M_CS + 1024;
constexpr int M_LDS = M_P + 4 * 64 * 272;

__global__ __launch_bounds__(256, 1) void k_mlp(char* __restrict__ ws, unsigned short* __restrict__ out) {
  __shared__ __attribute__((aligned(16))) char smem_[M_LDS];
  LAS char* smem = (LAS char*)smem_;
  const int tid = threadIdx.x, lane = tid & 63;
  const int w = __builtin_amdgcn_readfirstlane(tid >> 6);
  const int xcd = blockIdx.x & 7, idx = blockIdx.x >> 3;
  const int rt = xcd * 4 + idx / 7, j = idx % 7;
  STAMP_DECL
  STAMP(0);
  const u64 tag = load_epoch(ws);
  auto rH = __builtin_amdgcn_make_buffer_rsrc((void*)(ws + WS_HT), 0, L * D * 2, 0x00020000);
  auto rW1 = __builtin_amdgcn_make_buffer_rsrc((void*)(ws + WS_W1F), 0, D * F * 2, 0x00020000);
  auto rW2 = __builtin_amdgcn_make_buffer_rsrc((void*)(ws + WS_W2F), 0, D * F * 2, 0x00020000);
  auto rH1 = __builtin_amdgcn_make_buffer_rsrc((void*)(ws + WS_H1F), 0, L * F * 2, 0x00020000);
  const int l16 = lane * 16;
  u32x4 hv[14];
#pragma unroll
  for (int p = 0; p < 14; ++p) hv[p] = __builtin_amdgcn_raw_buffer_load_b128(rH, tid * 16, rt * (D * 128) + p * 4096, 0);
  SB();
  float csp[4];
  {
    auto rC = __builtin_amdgcn_make_buffer_rsrc((void*)(ws + WS_CS), 0, 4 * F * 4, 0x00020000);
#pragma unroll
    for (int k = 0; k < 4; ++k) csp[k] = __uint_as_float(__builtin_amdgcn_raw_buffer_load_b32(rC, lane * 4, (k * F + 256 * j + 64 * w) * 4, 0));
  }
  const int ft0 = j * 8 + w * 2;
  u32x4 wa[2][28];
  auto w1load = [&](int i) {
    const int ks = (((i / 7) + w) & 3) * 7 + (i % 7);
    wa[0][i] = __builtin_amdgcn_raw_buffer_load_b128(rW1, l16, (ft0 * 28 + ks) * 1024, 0);
    wa[1][i] = __builtin_amdgcn_raw_buffer_load_b128(rW1, l16, ((ft0 + 1) * 28 + ks) * 1024, 0);
  };
#pragma unroll
  for (int i = 0; i < WIN0; ++i) w1load(i);
  SB();
  {
    const int r = tid >> 3, c = tid & 7;
#pragma unroll
    for (int p = 0; p < 14; ++p) { const int row = 32 * p + r; *(LAS u32x4*)(smem + M_HT + row * 128 + ((c ^ (4 * ((row >> 1) & 1))) * 16)) = hv[p]; }
  }
  SB();
  STAMP(1);
  __syncthreads();
  STAMP(2);
  const int hh = lane >> 5, blk = (lane >> 4) & 1, q = (lane & 15) >> 2, p = lane & 3;
  const int sw = (q >> 1) & 1;
  const LAS char* b0 = smem + M_HT + (8 * hh + q) * 128 + ((2 * blk + (p >> 1) + 4 * sw) * 16) + (p & 1) * 8;
  const LAS char* b1 = smem + M_HT + (8 * hh + q) * 128 + ((2 * blk + (p >> 1) + 4 * (1 - sw)) * 16) + (p & 1) * 8;
  f32x16 acc[2][2];
#pragma unroll
  for (int a = 0; a < 2; ++a)
#pragma unroll
    for (int c = 0; c < 2; ++c)
#pragma unroll
      for (int e = 0; e < 16; ++e) acc[a][c][e] = 0.f;
  float s0 = 0.f, s1 = 0.f, q0 = 0.f, q1 = 0.f;
  const unsigned ONES = 0x3f803f80u;
  u32x4 wb[2][28];
  auto w2load = [&](int i) {
    const int n = i >> 2, fsub = i & 3;
    int jj = j + n; jj = jj >= 7 ? jj - 7 : jj;
    const int fs = 16 * jj + 4 * w + fsub;
    wb[0][i] = __builtin_amdgcn_raw_buffer_load_b128(rW2, l16, ((2 * j) * 112 + fs) * 1024, 0);
    wb[1][i] = __builtin_amdgcn_raw_buffer_load_b128(rW2, l16, ((2 * j + 1) * 112 + fs) * 1024, 0);
  };
  u32x4 f0, f1, n0, n1;
  auto bload = [&](int i, u32x4& o0, u32x4& o1) {
    const int go = ((((i / 7) + w) & 3) * 7 + (i % 7)) * 2048;
    o0 = tr_read2(b0 + go, b0 + go + 512);
    o1 = tr_read2(b1 + go, b1 + go + 512);
  };
  bload(0, f0, f1);
#pragma unroll
  for (int i = 0; i < 28; ++i) {
    if (i + 1 < 28) bload(i + 1, n0, n1);
    if (i < 5) { w1load(WIN0 + 3 * i); w1load(WIN0 + 3 * i + 1); w1load(WIN0 + 3 * i + 2); }
    else { const int n = WIN0 + 15 + (i - 5); if (n < 28) w1load(n); else if (n < 36) w2load(n - 28); }
    SB();
    acc[0][0] = mfma32b(wa[0][i], f0, acc[0][0]);
    acc[0][1] = mfma32b(wa[0][i], f1, acc[0][1]);
    acc[1][0] = mfma32b(wa[1][i], f0, acc[1][0]);
    acc[1][1] = mfma32b(wa[1][i], f1, acc[1][1]);
    if (i < 7) {
#pragma unroll
      for (int e = 0; e < 4; ++e) { s0 = dot2bf(f0[e], ONES, s0); q0 = dot2bf(f0[e], f0[e], q0); s1 = dot2bf(f1[e], ONES, s1); q1 = dot2bf(f1[e], f1[e], q1); }
    }
    SB();
    f0 = n0; f1 = n1;
  }
  STAMP(3);
  s0 += __shfl_xor(s0, 32); s1 += __shfl_xor(s1, 32); q0 += __shfl_xor(q0, 32); q1 += __shfl_xor(q1, 32);
  if (hh == 0) {
    *(LAS u32x2*)(smem + M_ST + (w * 64 + lane) * 8) = u32x2{__float_as_uint(s0), __float_as_uint(q0)};
    *(LAS u32x2*)(smem + M_ST + (w * 64 + 32 + lane) * 8) = u32x2{__float_as_uint(s1), __float_as_uint(q1)};
  }
  *(LAS float*)(smem + M_CS + (w * 64 + lane) * 4) = (csp[0] + csp[1]) + (csp[2] + csp[3]);
  __syncthreads();
  STAMP(4);
  float mr[2], ri[2];
#pragma unroll
  for (int c = 0; c < 2; ++c) {
    float s = 0.f, qq = 0.f;
#pragma unroll
    for (int ww = 0; ww < 4; ++ww) { u32x2 v = *(const LAS u32x2*)(smem + M_ST + (ww * 64 + c * 32 + (lane & 31)) * 8); s += __uint_as_float(v[0]); qq += __uint_as_float(v[1]); }
    const float mean = s * (1.0f / D);
    const float var = fmaxf(qq * (1.0f / D) - mean * mean, 0.f);
    float mb, sb; ln_scalars(mean, var, mb, sb);
    mr[c] = mb; ri[c] = rcp_(sb);
  }
  u32x4 own[2][4];
#pragma unroll
  for (int a = 0; a < 2; ++a) {
    float cs[16];
#pragma unroll
    for (int g4 = 0; g4 < 4; ++g4) {
      f32x4 v = *(const LAS f32x4*)(smem + M_CS + (w * 64 + a * 32 + 8 * g4 + 4 * hh) * 4);
#pragma unroll
      for (int e = 0; e < 4; ++e) cs[4 * g4 + e] = v[e];
    }
#pragma unroll
    for (int c = 0; c < 2; ++c) {
#pragma unroll
      for (int s = 0; s < 2; ++s) {
        u32x4 fr;
#pragma unroll
        for (int e = 0; e < 4; ++e) {
          const int r0 = 8 * s + 2 * e;
          const float v0 = (acc[a][c][r0] - mr[c] * cs[r0]) * ri[c], v1 = (acc[a][c][r0 + 1] - mr[c] * cs[r0 + 1]) * ri[c];
          fr[e] = pkbf(fmaxf(v0, 0.f), fmaxf(v1, 0.f));
        }
        own[c][2 * a + s] = fr;
        const int fs = 16 * j + 4 * w + 2 * a + s;
        __builtin_amdgcn_raw_buffer_store_b128(fr, rH1, l16, ((rt * 2 + c) * 112 + fs) * 1024, 16);
        w2load(8 + 4 * a + 2 * c + s);
        SB();
      }
    }
  }
  SB();
#pragma unroll
  for (int i = 16; i < 19; ++i) w2load(i);
  SB();
  asm volatile("s_waitcnt vmcnt(8)" ::: "memory");
  GAS u64* flg = (GAS u64*)(ws + WS_FLAGB) + (rt * 4 + w) * 8;
  if (lane == 0) __hip_atomic_store(flg + j, tag, __ATOMIC_RELAXED, __HIP_MEMORY_SCOPE_AGENT);
  SB();
#pragma unroll
  for (int i = 19; i < 28; ++i) w2load(i);
  SB();
  STAMP(5);
#pragma unroll
  for (int a = 0; a < 2; ++a)
#pragma unroll
    for (int c = 0; c < 2; ++c)
#pragma unroll
      for (int e = 0; e < 16; ++e) acc[a][c][e] = 0.f;
#pragma unroll
  for (int i = 0; i < 4; ++i) {
    acc[0][0] = mfma32b(wb[0][i], own[0][i], acc[0][0]);
    acc[0][1] = mfma32b(wb[0][i], own[1][i], acc[0][1]);
    acc[1][0] = mfma32b(wb[1][i], own[0][i], acc[1][0]);
    acc[1][1] = mfma32b(wb[1][i], own[1][i], acc[1][1]);
  }
  SB();
  {
    const int fl = lane < 7 ? lane : 0;
    for (unsigned spins = 0;; ++spins) {
      const u64 v = __hip_atomic_load(flg + fl, __ATOMIC_RELAXED, __HIP_MEMORY_SCOPE_AGENT);
      if (__all(v == tag)) break;
      if (spins > kSpinCap) { if (lane == 0) __hip_atomic_store((GAS unsigned*)(ws + WS_TMO), 2u, __ATOMIC_RELAXED, __HIP_MEMORY_SCOPE_AGENT); break; }
      __builtin_amdgcn_s_sleep(1);
    }
  }
  asm volatile("" ::: "memory");
  STAMP(6);
  u32x4 hf[2][24];
#pragma unroll
  for (int i = 4; i < 28; ++i) {
    const int n = i >> 2, fsub = i & 3;
    int jj = j + n; jj = jj >= 7 ? jj - 7 : jj;
    const int fs = 16 * jj + 4 * w + fsub;
    hf[0][i - 4] = __builtin_amdgcn_raw_buffer_load_b128(rH1, l16, ((rt * 2) * 112 + fs) * 1024, 16);
    hf[1][i - 4] = __builtin_amdgcn_raw_buffer_load_b128(rH1, l16, ((rt * 2 + 1) * 112 + fs) * 1024, 16);
  }
  SB();
#pragma unroll
  for (int i = 4; i < 28; ++i) {
    acc[0][0] = mfma32b(wb[0][i], hf[0][i - 4], acc[0][0]);
    acc[0][1] = mfma32b(wb[0][i], hf[1][i - 4], acc[0][1]);
    acc[1][0] = mfma32b(wb[1][i], hf[0][i - 4], acc[1][0]);
    acc[1][1] = mfma32b(wb[1][i], hf[1][i - 4], acc[1][1]);
    SB();
  }
  STAMP(7);
#pragma unroll
  for (int a = 0; a < 2; ++a)
#pragma unroll
    for (int c = 0; c < 2; ++c)
#pragma unroll
      for (int g4 = 0; g4 < 4; ++g4) {
        f32x4 v = {acc[a][c][4 * g4], acc[a][c][4 * g4 + 1], acc[a][c][4 * g4 + 2], acc[a][c][4 * g4 + 3]};
        *(LAS f32x4*)(smem + M_P + (w * 64 + c * 32 + (lane & 31)) * 272 + (a * 32 + 8 * g4 + 4 * hh) * 4) = v;
      }
  __syncthreads();
  STAMP(8);
  {
    const int l = lane, c4 = w;
    float o[16];
#pragma unroll
    for (int e = 0; e < 16; ++e) o[e] = 0.f;
#pragma unroll
    for (int ww = 0; ww < 4; ++ww)
#pragma unroll
      for (int i = 0; i < 4; ++i) {
        f32x4 v = *(const LAS f32x4*)(smem + M_P + (ww * 64 + l) * 272 + (16 * c4 + 4 * i) * 4);
#pragma unroll
        for (int e = 0; e < 4; ++e) o[4 * i + e] += v[e];
      }
    unsigned pk[8];
#pragma unroll
    for (int e = 0; e < 8; ++e) {
      const int r0 = 64 * j + 16 * c4 + 2 * e, r1 = r0 + 1;
      const float h0 = bf2f(*(const LAS unsigned short*)(smem + M_HT + r0 * 128 + (((l >> 3) ^ (4 * ((r0 >> 1) & 1))) * 16) + (l & 7) * 2));
      const float h1 = bf2f(*(const LAS unsigned short*)(smem + M_HT + r1 * 128 + (((l >> 3) ^ (4 * ((r1 >> 1) & 1))) * 16) + (l & 7) * 2));
      const unsigned ob = pkbf(o[2 * e], o[2 * e + 1]);
      pk[e] = pkbf(h0 + bflo(ob), h1 + bfhi(ob));
    }
    char* op = (char*)out + ((size_t)(rt * 64 + l) * D + 64 * j + 16 * c4) * 2;
    *(u32x4*)op = u32x4{pk[0], pk[1], pk[2], pk[3]};
    *(u32x4*)(op + 16) = u32x4{pk[4], pk[5], pk[6], pk[7]};
  }
  STAMP(9);
  STAMP_FLUSH(1);
}

}

extern "C" void kernel_launch(void* const* d_in, const int* in_sizes, int n_in, void* d_out, int out_size, void* d_ws, size_t ws_size, hipStream_t stream) {
  typedef const unsigned short* cp;
  k_ssm<<<256, 512, 0, stream>>>((cp)d_in[0], (cp)d_in[1], (cp)d_in[2], (cp)d_in[3], (cp)d_in[4], (cp)d_in[5], (cp)d_in[6], (cp)d_in[7], (cp)d_in[8], (cp)d_in[10], (cp)d_in[11], (char*)d_ws);
  k_mlp<<<224, 256, 0, stream>>>((char*)d_ws, (unsigned short*)d_out);
}
```

```cpp
#include <hip/hip_runtime.h>
#include <stdint.h>

namespace {

typedef __attribute__((ext_vector_type(8))) _Float16 f16x8;
typedef __attribute__((ext_vector_type(4))) _Float16 f16x4;
typedef __attribute__((ext_vector_type(2))) _Float16 f16x2;
typedef __attribute__((ext_vector_type(8))) __bf16 bf16x8;
typedef __attribute__((ext_vector_type(2))) __bf16 bf16x2;
typedef __attribute__((ext_vector_type(4))) float f32x4;
typedef __attribute__((ext_vector_type(16))) float f32x16;
typedef __attribute__((ext_vector_type(4))) unsigned u32x4;
typedef __attribute__((ext_vector_type(2))) unsigned u32x2;
typedef __attribute__((ext_vector_type(4))) short s16x4;
typedef unsigned long long u64;
#define LAS __attribute__((address_space(3)))
#define GAS __attribute__((address_space(1)))
#define DI __device__ __forceinline__

constexpr int L = 2048, D = 448, NM = 256, F = 1792;

constexpr size_t WS_EPOCH = 0;
constexpr size_t WS_TMO = 64;
constexpr size_t WS_FLAGB = 8192;
constexpr size_t WS_STATS = 16384;
constexpr size_t WS_XT = WS_STATS + 1024 * 16;
constexpr size_t WS_CS = WS_XT + (size_t)512 * D * 16;
constexpr size_t WS_HT = WS_CS + 4 * F * 4;
constexpr size_t WS_W1F = WS_HT + (size_t)D * L * 2;
constexpr size_t WS_W2F = WS_W1F + (size_t)D * F * 2;
constexpr size_t WS_H1F = WS_W2F + (size_t)D * F * 2;
constexpr size_t WS_END = WS_H1F + (size_t)L * F * 2;
constexpr size_t WS_STAMP = WS_END;
constexpr unsigned kSpinCap = 1u << 20;

#define STAMP(i) do {} while (0)
#define STAMP_DECL
#define STAMP_FLUSH(k) do {} while (0)
#define SB() __builtin_amdgcn_sched_barrier(0)
#define BAR() do { asm volatile("s_waitcnt lgkmcnt(0)" ::: "memory"); __builtin_amdgcn_s_barrier(); asm volatile("" ::: "memory"); } while (0)
DI float bf2f(unsigned b) { return __uint_as_float(b << 16); }
DI float bflo(unsigned p) { return __uint_as_float(p << 16); }
DI float bfhi(unsigned p) { return __uint_as_float(p & 0xffff0000u); }
DI unsigned pkbf(float a, float b) { bf16x2 r; r[0] = (__bf16)a; r[1] = (__bf16)b; return __builtin_bit_cast(unsigned, r); }
DI float rbf(float a) { return bflo(pkbf(a, a)); }
DI unsigned pkh(float a, float b) { f16x2 r; r[0] = (_Float16)a; r[1] = (_Float16)b; return __builtin_bit_cast(unsigned, r); }

template <int CTRL> DI float dppf(float v) { return __int_as_float(__builtin_amdgcn_update_dpp(0, __float_as_int(v), CTRL, 0xF, 0xF, true)); }
DI float rdl(float v, int l) { return __int_as_float(__builtin_amdgcn_readlane(__float_as_int(v), l)); }
DI float wave_sum(float v) {
  v += dppf<0xB1>(v); v += dppf<0x4E>(v); v += dppf<0x141>(v); v += dppf<0x140>(v);
  return (rdl(v, 0) + rdl(v, 16)) + (rdl(v, 32) + rdl(v, 48));
}
DI float wave_max(float v) {
  v = fmaxf(v, dppf<0xB1>(v)); v = fmaxf(v, dppf<0x4E>(v)); v = fmaxf(v, dppf<0x141>(v)); v = fmaxf(v, dppf<0x140>(v));
  return fmaxf(fmaxf(rdl(v, 0), rdl(v, 16)), fmaxf(rdl(v, 32), rdl(v, 48)));
}
DI s16x4 tr_read(const LAS char* p) { return __builtin_amdgcn_ds_read_tr16_b64_v4i16((LAS s16x4*)p); }
DI u32x4 tr_read2(const LAS char* p0, const LAS char* p1) {
  s16x4 a = tr_read(p0), b = tr_read(p1);
  u32x2 a2 = __builtin_bit_cast(u32x2, a), b2 = __builtin_bit_cast(u32x2, b);
  u32x4 r; r[0] = a2[0]; r[1] = a2[1]; r[2] = b2[0]; r[3] = b2[1]; return r;
}
DI f32x4 mfma16h(u32x4 a, u32x4 b, f32x4 c) { return __builtin_amdgcn_mfma_f32_16x16x32_f16(__builtin_bit_cast(f16x8, a), __builtin_bit_cast(f16x8, b), c, 0, 0, 0); }
DI f32x16 mfma32b(u32x4 a, u32x4 b, f32x16 c) { return __builtin_amdgcn_mfma_f32_32x32x16_bf16(__builtin_bit_cast(bf16x8, a), __builtin_bit_cast(bf16x8, b), c, 0, 0, 0); }
DI float dot2bf(unsigned a, unsigned b, float c) { return __builtin_amdgcn_fdot2_f32_bf16(__builtin_bit_cast(bf16x2, a), __builtin_bit_cast(bf16x2, b), c, false); }

DI u64 load_epoch(const char* ws) {
  const u32x2 v = *(const u32x2*)(ws + WS_EPOCH);
  unsigned lo = __builtin_amdgcn_readfirstlane(v[0]), hi = __builtin_amdgcn_readfirstlane(v[1]);
  asm volatile("" : "+s"(lo), "+s"(hi));
  return ((u64)hi << 32) | lo;
}
template <int I> struct IC { static constexpr int value = I; };
template <int B, int E, class Fn> DI void static_for(Fn&& fn) { if constexpr (B < E) { fn(IC<B>{}); static_for<B + 1, E>(fn); } }
struct cf { float r, i; };
DI cf cmul(cf a, cf b) { cf o; o.r = a.r * b.r - a.i * b.i; o.i = a.r * b.i + a.i * b.r; return o; }
DI cf csq(cf a) { cf o; o.r = a.r * a.r - a.i * a.i; o.i = 2.f * a.r * a.i; return o; }
DI cf cexp_p2(float zr2, float zf, float p2) {
  float m = __builtin_amdgcn_exp2f(zr2 * p2);
  float t = __builtin_amdgcn_fractf(zf * p2);
  cf o; o.r = m * __builtin_amdgcn_cosf(t); o.i = m * __builtin_amdgcn_sinf(t); return o;
}

DI void ln_scalars(float mean, float var, float& mb, float& sb) {
  mb = rbf(mean);
  float vb = rbf(var);
  sb = rbf(__builtin_sqrtf(rbf(vb + 1e-5f)));
}
DI float rcp_(float s) { return __builtin_amdgcn_rcpf(s); }

struct IdxTab { unsigned short v[256][12]; };
constexpr int bf16_round_index(int lag) {
  if (lag < 0 || lag > 2047) return 641;
  if (lag < 256) return lag;
  int e = 8; while ((lag >> (e + 1)) != 0) ++e;
  const int sh = e - 7;
  int q = lag >> sh; const int r = lag & ((1 << sh) - 1), half = 1 << (sh - 1);
  if (r > half || (r == half && (q & 1))) ++q;
  return 256 + (e - 8) * 128 + (q - 128);
}
constexpr IdxTab make_idx() {
  IdxTab t{};
  for (int c = 0; c < 256; ++c) { for (int e = 0; e < 11; ++e) t.v[c][e] = (unsigned short)(2 * bf16_round_index(2048 - 8 * c + 3 - e)); t.v[c][11] = 0; }
  return t;
}
__device__ const IdxTab g_idx = make_idx();

constexpr int A_TAB = 0;
constexpr int A_MISC = A_TAB + 8 * 16384;
constexpr int A_KDC = A_MISC;
constexpr int A_SC = A_KDC + 2 * 1312;
constexpr int A_XS = A_SC + 128;
constexpr int A_LDS = A_XS + 9216 + 64;
constexpr int TOE_CP = 4160, TOE_CH = 4 * TOE_CP;
constexpr int A_U16 = A_TAB + 4096;
constexpr int A_TOE2 = A_TAB + 13312;
constexpr int A_YP = A_TAB + 46592;

__global__ __launch_bounds__(512, 2) void k_ssm(const unsigned short* __restrict__ x, const unsigned short* __restrict__ Are, const unsigned short* __restrict__ Aim,
                                                 const unsigned short* __restrict__ Bre, const unsigned short* __restrict__ Bim, const unsigned short* __restrict__ Cre,
                                                 const unsigned short* __restrict__ Cim, const unsigned short* __restrict__ logdt, const unsigned short* __restrict__ Dsk,
                                                 const unsigned short* __restrict__ w1, const unsigned short* __restrict__ w2, char* __restrict__ ws) {
  __shared__ __attribute__((aligned(16))) char smem_[A_LDS];
  LAS char* smem = (LAS char*)smem_;
  const int tid = threadIdx.x, lane = tid & 63;
  const int w = __builtin_amdgcn_readfirstlane(tid >> 6);
  const int b = blockIdx.x;
  const bool has_ch = b < 224;
  const int c2 = w >> 2, w4 = w & 3;
  const int ch = 2 * b + c2;
  STAMP_DECL
  STAMP(0);
  const int bs = has_ch ? b : 0;
  const int chs = 2 * bs + c2;
  const int po = chs * NM + w4 * 64 + lane;
  const unsigned short r_are = Are[po], r_aim = Aim[po], r_bre = Bre[po], r_bim = Bim[po], r_cre = Cre[po], r_cim = Cim[po];
  const unsigned short r_ldt = logdt[chs];
  const u32x2 ev = *(const u32x2*)(ws + WS_EPOCH);
  SB();
  const int row = 8 * b + w;
  u32x4 xv = *(const u32x4*)(x + (size_t)row * D + min(lane, 55) * 8);
  const int trt = bs & 31, tg = bs >> 5;
  const u32x4 xt = *(const u32x4*)(x + (size_t)(64 * trt + 8 * w + (lane >> 3)) * D + 64 * tg + (lane & 7) * 8);
  const unsigned r_dsk = *(const unsigned*)(Dsk + 2 * bs);
  const int t = tid & 255;
  const u32x4 ix0 = *(const u32x4*)&g_idx.v[t][0];
  const u32x2 ix1 = *(const u32x2*)&g_idx.v[t][8];
  SB();
  unsigned elo = __builtin_amdgcn_readfirstlane(ev[0]), ehi = __builtin_amdgcn_readfirstlane(ev[1]);
  asm volatile("" : "+s"(elo), "+s"(ehi));
  const u64 tag = (((u64)ehi << 32) | elo) + 1;
  const unsigned tlo_ = (unsigned)tag, thi_ = (unsigned)(tag >> 32);
  LAS char* tab = smem + A_TAB + w * 16384;

  auto ln_row = [&]() {
    if (lane >= 56) xv = u32x4{0u, 0u, 0u, 0u};
    float xf[8];
#pragma unroll
    for (int j = 0; j < 4; ++j) { xf[2 * j] = bflo(xv[j]); xf[2 * j + 1] = bfhi(xv[j]); }
    float s = 0.f;
#pragma unroll
    for (int j = 0; j < 8; ++j) s += xf[j];
    const float mean = wave_sum(s) * (1.0f / D);
    float q = 0.f;
#pragma unroll
    for (int j = 0; j < 8; ++j) { float d = xf[j] - mean; q += d * d; }
    if (lane >= 56) q = 0.f;
    const float var = wave_sum(q) * (1.0f / D);
    float mb, sb; ln_scalars(mean, var, mb, sb);
    if (lane == 0) *(LAS unsigned*)(smem + A_XS + 9216 + w * 4) = (__float_as_uint(mb) >> 16) | (__float_as_uint(sb) & 0xffff0000u);
    if (has_ch) *(LAS u32x4*)(smem + A_XS + (8 * w + (lane >> 3)) * 144 + (lane & 7) * 16) = xt;
  };
  auto ux_publish = [&]() {
    if (tid < 4) {
      const u32x2 p = *(const LAS u32x2*)(smem + A_XS + 9216 + tid * 8);
      auto rs = __builtin_amdgcn_make_buffer_rsrc((void*)(ws + WS_STATS), 0, 1024 * 16, 0x00020000);
      __builtin_amdgcn_raw_buffer_store_b128(u32x4{p[0], p[1], tlo_, thi_}, rs, (4 * b + tid) * 16, 0, 16);
    }
    if (has_ch) {
      auto rx = __builtin_amdgcn_make_buffer_rsrc((void*)(ws + WS_XT), 0, 512 * D * 16, 0x00020000);
      const int qd = lane & 15;
#pragma unroll
      for (int i = 0; i < 2; ++i) {
        const int c = 8 * w + 4 * i + (lane >> 4);
        const LAS char* a0 = smem + A_XS + (4 * qd) * 144 + c * 2;
        const unsigned r0 = *(const LAS unsigned short*)a0, r1 = *(const LAS unsigned short*)(a0 + 144), r2 = *(const LAS unsigned short*)(a0 + 288), r3 = *(const LAS unsigned short*)(a0 + 432);
        __builtin_amdgcn_raw_buffer_store_b128(u32x4{r0 | (r1 << 16), r2 | (r3 << 16), tlo_, thi_}, rx, ((64 * tg + c) * 512 + 16 * trt + qd) * 16, 0, 16);
      }
    }
  };
  STAMP(1);

  if (!has_ch) {
    ln_row();
    BAR();
    ux_publish();
    const int task = (b - 224) * 8 + w;
    if (task < 224) {
      const bool is_w1 = task < 112;
      const int tk = is_w1 ? task : task - 112;
      const int nb = is_w1 ? tk % 28 : tk % 7, kg = is_w1 ? tk / 28 : tk / 7;
      const int rld = is_w1 ? F : D;
      const unsigned short* src = (is_w1 ? w1 : w2) + (size_t)(16 * 7 * kg + (lane >> 3)) * rld + 64 * nb + (lane & 7) * 8;
      u32x4 r0[7], r1[7];
#pragma unroll
      for (int u = 0; u < 7; ++u) { r0[u] = *(const u32x4*)(src + (size_t)(16 * u) * rld); r1[u] = *(const u32x4*)(src + (size_t)(16 * u + 8) * rld); }
      LAS char* sc = smem + w * 16384;
#pragma unroll
      for (int u = 0; u < 7; ++u) { *(LAS u32x4*)(sc + u * 2048 + lane * 16) = r0[u]; *(LAS u32x4*)(sc + u * 2048 + 1024 + lane * 16) = r1[u]; }
      __builtin_amdgcn_s_waitcnt(0xC07F);
      __builtin_amdgcn_wave_barrier();
      const int g = lane >> 4, blk = g & 1, hh = g >> 1, q = (lane & 15) >> 2, p = lane & 3;
      const int q0 = is_w1 ? (8 * hh + q) : (4 * hh + q);
      const int q1 = is_w1 ? (8 * hh + 4 + q) : (8 + 4 * hh + q);
      auto rd = __builtin_amdgcn_make_buffer_rsrc((void*)(ws + (is_w1 ? WS_W1F : WS_W2F)), 0, D * F * 2, 0x00020000);
      const int nks = is_w1 ? 28 : 112;
      float cs[2] = {0.f, 0.f};
#pragma unroll
      for (int u = 0; u < 7; ++u) {
#pragma unroll
        for (int tt = 0; tt < 2; ++tt) {
          const int cb = (tt * 32 + blk * 16 + 4 * p) * 2;
          u32x4 fr = tr_read2(sc + u * 2048 + q0 * 128 + cb, sc + u * 2048 + q1 * 128 + cb);
          __builtin_amdgcn_raw_buffer_store_b128(fr, rd, lane * 16, ((2 * nb + tt) * nks + 7 * kg + u) * 1024, 16);
#pragma unroll
          for (int e = 0; e < 4; ++e) cs[tt] = dot2bf(fr[e], 0x3f803f80u, cs[tt]);
        }
      }
      if (is_w1) {
        cs[0] += __shfl_xor(cs[0], 32); cs[1] += __shfl_xor(cs[1], 32);
        auto rc = __builtin_amdgcn_make_buffer_rsrc((void*)(ws + WS_CS), 0, 4 * F * 4, 0x00020000);
        __builtin_amdgcn_raw_buffer_store_b32(__float_as_uint(lane < 32 ? cs[0] : cs[1]), rc, (kg * F + 64 * nb + lane) * 4, 0, 16);
      }
    }
    return;
  }

  STAMP(13);
  float k2048;
  int ew;
  {
    const float are = bf2f(r_are), aim = bf2f(r_aim), bre = bf2f(r_bre), bim = bf2f(r_bim), cre = bf2f(r_cre), cim = bf2f(r_cim);
    const float dt = rbf(expf(bf2f(r_ldt)));
    const float zr = dt * are, zi = dt * aim;
    const float zr2 = zr * 1.44269504088896341f, zf = zi * 0.15915494309189535f;
    const float dbr = dt * bre, dbi = dt * bim;
    cf CB; CB.r = cre * dbr - cim * dbi; CB.i = cre * dbi + cim * dbr;
    const float mx = wave_max(fmaxf(fabsf(CB.r), fabsf(CB.i)));
    const float gz = fmaxf(wave_max(zr2), 0.f) * 2048.f;
    int e = (mx > 0.f) ? ((int)(__float_as_uint(mx) >> 23) - 127) : 0;
    e += (int)ceilf(fminf(gz, 100.f));
    e = max(-120, min(120, e));
    ew = e;
    const float S = __uint_as_float((unsigned)(127 - e) << 23);
    cf G0; G0.r = CB.r * S; G0.i = CB.i * S;
    const cf E1 = cexp_p2(zr2, zf, 1.f), E2 = csq(E1), E4 = cexp_p2(zr2, zf, 4.f), E8 = csq(E4);
    const cf E16 = cexp_p2(zr2, zf, 16.f), E32 = csq(E16), E64 = cexp_p2(zr2, zf, 64.f), E128 = csq(E64);
    const cf E256 = cexp_p2(zr2, zf, 256.f), E512 = csq(E256), E1024 = cexp_p2(zr2, zf, 1024.f), E2048 = csq(E1024);
    STAMP(14);
    SB();
    ln_row();
    SB();
    const int sw = (lane >> 1) & 3;
    LAS char* tb = tab + lane * 64;
    auto put = [&](int tile, int half, const cf* v, float isign) {
      u32x4 re, im;
#pragma unroll
      for (int j = 0; j < 4; ++j) { re[j] = pkh(v[2 * j].r, v[2 * j + 1].r); im[j] = pkh(isign * v[2 * j].i, isign * v[2 * j + 1].i); }
      *(LAS u32x4*)(tb + tile * 4096 + ((half ^ sw) * 16)) = re;
      *(LAS u32x4*)(tb + tile * 4096 + (((2 + half) ^ sw) * 16)) = im;
    };
    cf Er[8];
    Er[0].r = 1.f; Er[0].i = 0.f; Er[1] = E1; Er[2] = E2; Er[3] = cmul(E1, E2); Er[4] = E4; Er[5] = cmul(E4, E1); Er[6] = cmul(E4, E2); Er[7] = cmul(E4, Er[3]);
    {
      cf Y[8];
      Y[0].r = 1.f; Y[0].i = 0.f; Y[1] = E8; Y[2] = E16; Y[3] = cmul(E8, E16); Y[4] = E32; Y[5] = cmul(E32, E8); Y[6] = cmul(E32, E16); Y[7] = cmul(E32, Y[3]);
      put(3, 0, Y, 1.f);
      cf Y2[8];
      Y2[0] = E64;
#pragma unroll
      for (int j = 1; j < 8; ++j) Y2[j] = cmul(Y[j], E64);
      put(3, 1, Y2, 1.f);
    }
    STAMP(15);
    cf G[8];
    G[0] = G0; G[1] = cmul(G0, E128); G[2] = cmul(G0, E256); G[3] = cmul(G[1], E256);
#pragma unroll
    for (int j = 0; j < 4; ++j) G[4 + j] = cmul(G[j], E512);
    {
      cf T[8];
      T[0] = G[0];
#pragma unroll
      for (int j = 1; j < 8; ++j) T[j] = cmul(G[0], Er[j]);
      put(0, 0, T, -1.f);
      SB();
      BAR();
      ux_publish();
      SB();
      T[0] = G[1];
#pragma unroll
      for (int j = 1; j < 8; ++j) T[j] = cmul(G[1], Er[j]);
      put(0, 1, T, -1.f);
    }
    {
      cf T[8];
      T[0] = G[2]; T[1] = cmul(G[2], E2); T[2] = cmul(G[2], E4); T[3] = cmul(G[2], Er[6]);
      T[4] = G[3]; T[5] = cmul(G[3], E2); T[6] = cmul(G[3], E4); T[7] = cmul(G[3], Er[6]);
      put(1, 0, T, -1.f);
#pragma unroll
      for (int j = 0; j < 4; ++j) { T[2 * j] = G[4 + j]; T[2 * j + 1] = cmul(G[4 + j], E4); }
      put(1, 1, T, -1.f);
    }
    {
      cf T[8];
#pragma unroll
      for (int j = 0; j < 8; ++j) T[j] = cmul(G[j], E1024);
      put(2, 0, T, -1.f);
    }
    k2048 = wave_sum(G0.r * E2048.r - G0.i * E2048.i);
  }
  STAMP(2);
  __builtin_amdgcn_s_waitcnt(0xC07F);
  __builtin_amdgcn_wave_barrier();
  {
    const int g = lane >> 4, q = (lane & 15) >> 2, p = lane & 3;
    f32x4 acc0 = {0.f, 0.f, 0.f, 0.f}, acc1 = acc0, acc2 = acc0;
    const int part = q & 1, cl = part * 2 + (p >> 1);
    u32x4 bb[4], a0[4], a1[4], a2[4];
#pragma unroll
    for (int s = 0; s < 4; ++s) {
      const int m0 = 16 * s + 8 * (g >> 1) + 2 * (g & 1) + (q >> 1), m1 = m0 + 4;
      const int o0 = m0 * 64 + ((cl ^ ((m0 >> 1) & 3)) * 16) + (p & 1) * 8;
      const int o1 = m1 * 64 + ((cl ^ ((m1 >> 1) & 3)) * 16) + (p & 1) * 8;
      bb[s] = tr_read2(tab + 3 * 4096 + o0, tab + 3 * 4096 + o1);
      a0[s] = tr_read2(tab + o0, tab + o1);
      a1[s] = tr_read2(tab + 4096 + o0, tab + 4096 + o1);
      a2[s] = tr_read2(tab + 8192 + o0, tab + 8192 + o1);
    }
    SB();
#pragma unroll
    for (int s = 0; s < 4; ++s) { acc0 = mfma16h(a0[s], bb[s], acc0); acc1 = mfma16h(a1[s], bb[s], acc1); acc2 = mfma16h(a2[s], bb[s], acc2); }
    SB();
    *(LAS f32x4*)(tab + lane * 16) = acc0;
    *(LAS f32x4*)(tab + 1024 + lane * 16) = acc1;
    *(LAS f32x4*)(tab + 2048 + lane * 16) = acc2;
    if (lane == 0) { *(LAS int*)(smem + A_SC + w * 8) = ew; *(LAS float*)(smem + A_SC + w * 8 + 4) = k2048; }
  }
  STAMP(3);
  auto rst = __builtin_amdgcn_make_buffer_rsrc((void*)(ws + WS_STATS), 0, 1024 * 16, 0x00020000);
  auto rxq = __builtin_amdgcn_make_buffer_rsrc((void*)(ws + WS_XT), 0, 512 * D * 16, 0x00020000);
  u32x4 gr[4];
  auto sweep = [&]() {
    gr[0] = __builtin_amdgcn_raw_buffer_load_b128(rst, tid * 32, 0, 16);
    gr[1] = __builtin_amdgcn_raw_buffer_load_b128(rst, tid * 32 + 16, 0, 16);
    gr[2] = __builtin_amdgcn_raw_buffer_load_b128(rxq, tid * 16, (2 * b) * 8192, 16);
    gr[3] = __builtin_amdgcn_raw_buffer_load_b128(rxq, tid * 16, (2 * b + 1) * 8192, 16);
  };
  sweep();
  BAR();
  STAMP(4);
  float SKinv;
  {
    int e4[4]; float k4[4];
#pragma unroll
    for (int j = 0; j < 4; ++j) { e4[j] = *(LAS int*)(smem + A_SC + (c2 * 4 + j) * 8); k4[j] = *(LAS float*)(smem + A_SC + (c2 * 4 + j) * 8 + 4); }
    const int em = max(max(e4[0], e4[1]), max(e4[2], e4[3]));
    float fw[4];
#pragma unroll
    for (int j = 0; j < 4; ++j) { int d = max(e4[j] - em + 5, -120); fw[j] = __uint_as_float((unsigned)(127 + d) << 23); }
    SKinv = __uint_as_float((unsigned)(127 + em - 5) << 23);
    LAS char* kdc = smem + A_KDC + c2 * 1312;
    if (t < 192) {
      const int tile = t >> 6, ln = t & 63, c = ln & 15, g = ln >> 4;
      f32x4 s = {0.f, 0.f, 0.f, 0.f};
#pragma unroll
      for (int j = 0; j < 4; ++j) { f32x4 v = *(LAS f32x4*)(smem + A_TAB + (c2 * 4 + j) * 16384 + tile * 1024 + ln * 16); s += v * fw[j]; }
      const unsigned p01 = pkh(s[0], s[1]), p23 = pkh(s[2], s[3]);
      if (tile == 0) {
        u32x2 v = {p01, p23}; *(LAS u32x2*)(kdc + (128 * (g >> 1) + 8 * c + 4 * (g & 1)) * 2) = v;
      } else if (tile == 1) {
        if (g < 2) { u32x2 v = {p01, p23}; *(LAS u32x2*)(kdc + (256 + 64 * g + 4 * c) * 2) = v; }
        else { const int i0 = 384 + 64 * (g - 2) + 2 * c; *(LAS unsigned*)(kdc + i0 * 2) = p01; *(LAS unsigned*)(kdc + (i0 + 32) * 2) = p23; }
      } else if (g < 2) {
        const int i0 = 512 + 64 * g + c;
        *(LAS unsigned short*)(kdc + i0 * 2) = (unsigned short)p01; *(LAS unsigned short*)(kdc + (i0 + 16) * 2) = (unsigned short)(p01 >> 16);
        *(LAS unsigned short*)(kdc + (i0 + 32) * 2) = (unsigned short)p23; *(LAS unsigned short*)(kdc + (i0 + 48) * 2) = (unsigned short)(p23 >> 16);
      }
    } else if (t == 192) {
      const float k = k4[0] * fw[0] + k4[1] * fw[1] + k4[2] * fw[2] + k4[3] * fw[3];
      *(LAS unsigned*)(kdc + 640 * 2) = pkh(k, 0.f);
    }
  }
  float SKo;
  {
    int e4[4];
#pragma unroll
    for (int j = 0; j < 4; ++j) e4[j] = *(LAS int*)(smem + A_SC + ((1 - c2) * 4 + j) * 8);
    const int em = max(max(e4[0], e4[1]), max(e4[2], e4[3]));
    SKo = __uint_as_float((unsigned)(127 + em - 5) << 23);
  }
  const float SK0 = c2 ? SKo : SKinv, SK1 = c2 ? SKinv : SKo;
  unsigned ub[4], xc[4];
  {
    for (unsigned spins = 0;; ++spins) {
      bool ok = true;
#pragma unroll
      for (int i = 0; i < 4; ++i) ok = ok && gr[i][2] == tlo_ && gr[i][3] == thi_;
      if (__all(ok)) break;
      if (spins > kSpinCap) { if (lane == 0) __hip_atomic_store((GAS unsigned*)(ws + WS_TMO), 1u, __ATOMIC_RELAXED, __HIP_MEMORY_SCOPE_AGENT); break; }
      sweep();
    }
    xc[0] = (gr[2][0] & 0xffffu) | (gr[3][0] << 16); xc[1] = (gr[2][0] >> 16) | (gr[3][0] & 0xffff0000u);
    xc[2] = (gr[2][1] & 0xffffu) | (gr[3][1] << 16); xc[3] = (gr[2][1] >> 16) | (gr[3][1] & 0xffff0000u);
    float u0[4], u1[4];
#pragma unroll
    for (int i = 0; i < 4; ++i) {
      const unsigned pay = gr[i >> 1][i & 1];
      const float mb = bflo(pay), ri = rcp_(bfhi(pay));
      const unsigned tt = pkbf(bflo(xc[i]) - mb, bfhi(xc[i]) - mb);
      ub[i] = pkbf(bflo(tt) * ri, bfhi(tt) * ri);
      u0[i] = bflo(ub[i]); u1[i] = bfhi(ub[i]);
    }
    *(LAS u32x2*)(smem + A_U16 + 512 + tid * 8) = u32x2{pkh(u0[0], u0[1]), pkh(u0[2], u0[3])};
    *(LAS u32x2*)(smem + A_U16 + 4608 + 512 + tid * 8) = u32x2{pkh(u1[0], u1[1]), pkh(u1[2], u1[3])};
    if (t < 32) *(LAS u32x4*)(smem + A_U16 + c2 * 4608 + t * 16) = u32x4{0u, 0u, 0u, 0u};
  }
  STAMP(5);
  BAR();
  u32x4 Bf[2][8];
  {
    const int bq = lane & 15, g = lane >> 4;
    const LAS char* ua = smem + A_U16 + c2 * 4608 + (256 + 16 * (bq - 1) + 8 * g) * 2 - 64 * w4 - 256;
#pragma unroll
    for (int it = 0; it < 2; ++it)
#pragma unroll
      for (int k = 0; k < 8; ++k) Bf[it][k] = *(const LAS u32x4*)(ua + k * 512 + (1 - it) * 256);
  }
  STAMP(6);
  {
    const LAS char* kdc = smem + A_KDC + c2 * 1312;
    LAS char* toe = smem + A_TOE2 + c2 * TOE_CH;
    unsigned v[11];
#pragma unroll
    for (int e = 0; e < 11; ++e) {
      const unsigned wd = e < 8 ? ix0[e >> 1] : ix1[(e - 8) >> 1];
      const unsigned off = (e & 1) ? (wd >> 16) : (wd & 0xffffu);
      v[e] = *(const LAS unsigned short*)(kdc + off);
    }
    unsigned ev_[5], od[5];
#pragma unroll
    for (int j = 0; j < 5; ++j) { ev_[j] = v[2 * j] | (v[2 * j + 1] << 16); od[j] = v[2 * j + 1] | (v[2 * j + 2] << 16); }
    *(LAS u32x4*)(toe + 0 * TOE_CP + t * 16) = u32x4{od[1], od[2], od[3], od[4]};
    *(LAS u32x4*)(toe + 1 * TOE_CP + t * 16) = u32x4{ev_[1], ev_[2], ev_[3], ev_[4]};
    *(LAS u32x4*)(toe + 2 * TOE_CP + t * 16) = u32x4{od[0], od[1], od[2], od[3]};
    *(LAS u32x4*)(toe + 3 * TOE_CP + t * 16) = u32x4{ev_[0], ev_[1], ev_[2], ev_[3]};
    if (t < 12) {
      const int s = t & 3, c = 256 + (t >> 2);
      const u32x2 k03 = *(const LAS u32x2*)kdc;
      const unsigned k0 = k03[0] & 0xffffu, k1 = k03[0] >> 16, k2 = k03[1] & 0xffffu, k3 = k03[1] >> 16;
      unsigned d0 = s == 0 ? k0 : s == 1 ? (k1 | (k0 << 16)) : s == 2 ? (k2 | (k1 << 16)) : (k3 | (k2 << 16));
      unsigned d1 = s == 2 ? k0 : s == 3 ? (k1 | (k0 << 16)) : 0u;
      if (c != 256) { d0 = 0u; d1 = 0u; }
      *(LAS u32x4*)(toe + s * TOE_CP + c * 16) = u32x4{d0, d1, 0u, 0u};
    }
  }
  STAMP(7);
  STAMP(8);
  BAR();
  STAMP(9);
  if (b == 0 && tid == 0) __hip_atomic_store((GAS u64*)(ws + WS_EPOCH), tag, __ATOMIC_RELAXED, __HIP_MEMORY_SCOPE_AGENT);
  {
    const int bq = lane & 15, g = lane >> 4;
    f32x4 acc[8];
#pragma unroll
    for (int i = 0; i < 8; ++i) acc[i] = f32x4{0.f, 0.f, 0.f, 0.f};
    const LAS char* ta = smem + A_TOE2 + c2 * TOE_CH + (bq & 3) * TOE_CP + (2032 - 4 * (bq >> 2) + 8 * g) * 2 - 64 * w4 - 512 * 7 - 256;
    u32x4 A[2][8];
#pragma unroll
    for (int it = 0; it < 2; ++it)
#pragma unroll
      for (int k = 0; k < 8; ++k) {
        const u32x2 lo = *(const volatile LAS u32x2*)(ta + (7 - k) * 512 + (1 - it) * 256), hi = *(const volatile LAS u32x2*)(ta + (7 - k) * 512 + (1 - it) * 256 + 8);
        A[it][k] = u32x4{lo[0], lo[1], hi[0], hi[1]};
      }
    SB();
#pragma unroll
    for (int it = 0; it < 2; ++it) {
#pragma unroll
      for (int k = 0; k < 8; ++k)
#pragma unroll
        for (int i = k; i < 8; ++i) acc[i] = mfma16h(A[it][k], Bf[it][i - k], acc[i]);
      SB();
    }
    LAS char* yp = smem + A_YP + w * 8192;
#pragma unroll
    for (int i = 0; i < 8; ++i) *(LAS f32x4*)(yp + i * 1024 + bq * 64 + g * 16) = acc[i];
  }
  STAMP(10);
  BAR();
  STAMP(11);
  {
    f32x4 y0 = {0.f, 0.f, 0.f, 0.f}, y1 = y0;
#pragma unroll
    for (int j = 0; j < 4; ++j) {
      y0 += *(const LAS f32x4*)(smem + A_YP + j * 8192 + tid * 16);
      y1 += *(const LAS f32x4*)(smem + A_YP + (4 + j) * 8192 + tid * 16);
    }
    const float d0 = bflo(r_dsk), d1 = bfhi(r_dsk);
    unsigned hp[4];
#pragma unroll
    for (int i = 0; i < 4; ++i) {
      const unsigned yb = pkbf(y0[i] * SK0, y1[i] * SK1);
      const unsigned sk = pkbf(bflo(ub[i]) * d0, bfhi(ub[i]) * d1);
      const unsigned y2 = pkbf(bflo(yb) + bflo(sk), bfhi(yb) + bfhi(sk));
      hp[i] = pkbf(bflo(xc[i]) + bflo(y2), bfhi(xc[i]) + bfhi(y2));
    }
    const u32x2 h0 = {(hp[0] & 0xffffu) | (hp[1] << 16), (hp[2] & 0xffffu) | (hp[3] << 16)};
    const u32x2 h1 = {(hp[0] >> 16) | (hp[1] & 0xffff0000u), (hp[2] >> 16) | (hp[3] & 0xffff0000u)};
    auto rh = __builtin_amdgcn_make_buffer_rsrc((void*)(ws + WS_HT), 0, L * D * 2, 0x00020000);
    const int off = (tid >> 4) * (D * 128) + (2 * b) * 128 + (tid & 15) * 8;
    __builtin_amdgcn_raw_buffer_store_b64(h0, rh, off, 0, 16);
    __builtin_amdgcn_raw_buffer_store_b64(h1, rh, off + 128, 0, 16);
  }
  STAMP(12);
  STAMP_FLUSH(0);
}

constexpr int WIN0 = 14, WIN = 34;
constexpr int win_tgt(int t) { if (t < 0) return WIN0; int a = WIN0 + 3 * (t + 1), b = t + 1 + WIN; int m = a < b ? a : b; return m > 68 ? 68 : m; }
constexpr int M_HT = 0;
constexpr int M_ST = 57344;
constexpr int M_CS = M_ST + 2048;
constexpr int M_P = M_CS + 1024;
constexpr int M_LDS = M_P + 4 * 64 * 272;

__global__ __launch_bounds__(256, 1) void k_mlp(char* __restrict__ ws, unsigned short* __restrict__ out) {
  __shared__ __attribute__((aligned(16))) char smem_[M_LDS];
  LAS char* smem = (LAS char*)smem_;
  const int tid = threadIdx.x, lane = tid & 63;
  const int w = __builtin_amdgcn_readfirstlane(tid >> 6);
  const int xcd = blockIdx.x & 7, idx = blockIdx.x >> 3;
  const int rt = xcd * 4 + idx / 7, j = idx % 7;
  STAMP_DECL
  STAMP(0);
  const u64 tag = load_epoch(ws);
  auto rH = __builtin_amdgcn_make_buffer_rsrc((void*)(ws + WS_HT), 0, L * D * 2, 0x00020000);
  auto rW1 = __builtin_amdgcn_make_buffer_rsrc((void*)(ws + WS_W1F), 0, D * F * 2, 0x00020000);
  auto rW2 = __builtin_amdgcn_make_buffer_rsrc((void*)(ws + WS_W2F), 0, D * F * 2, 0x00020000);
  auto rH1 = __builtin_amdgcn_make_buffer_rsrc((void*)(ws + WS_H1F), 0, L * F * 2, 0x00020000);
  const int l16 = lane * 16;
  u32x4 hv[14];
#pragma unroll
  for (int p = 0; p < 14; ++p) hv[p] = __builtin_amdgcn_raw_buffer_load_b128(rH, tid * 16, rt * (D * 128) + p * 4096, 0);
  SB();
  float csp[4];
  {
    auto rC = __builtin_amdgcn_make_buffer_rsrc((void*)(ws + WS_CS), 0, 4 * F * 4, 0x00020000);
#pragma unroll
    for (int k = 0; k < 4; ++k) csp[k] = __uint_as_float(__builtin_amdgcn_raw_buffer_load_b32(rC, lane * 4, (k * F + 256 * j + 64 * w) * 4, 0));
  }
  const int ft0 = j * 8 + w * 2;
  u32x4 wa[56], wb[2][28];
  auto seqload = [&](int n) {
    if (n < 56) {
      const int a = n / 28, i = n % 28;
      const int ks = (((i / 7) + w) & 3) * 7 + (i % 7);
      wa[n] = __builtin_amdgcn_raw_buffer_load_b128(rW1, l16, ((ft0 + a) * 28 + ks) * 1024, 0);
    } else {
      const int m = n - 56, i = m >> 1, dt = m & 1;
      int jj = j + (i >> 2); jj = jj >= 7 ? jj - 7 : jj;
      const int fs = 16 * jj + 4 * w + (i & 3);
      wb[dt][i] = __builtin_amdgcn_raw_buffer_load_b128(rW2, l16, ((2 * j + dt) * 112 + fs) * 1024, 0);
    }
  };
#pragma unroll
  for (int n = 0; n < WIN0; ++n) seqload(n);
  SB();
  {
    const int r = tid >> 3, c = tid & 7;
#pragma unroll
    for (int p = 0; p < 14; ++p) { const int row = 32 * p + r; *(LAS u32x4*)(smem + M_HT + row * 128 + ((c ^ (4 * ((row >> 1) & 1))) * 16)) = hv[p]; }
  }
  *(LAS float*)(smem + M_CS + (w * 64 + lane) * 4) = (csp[0] + csp[1]) + (csp[2] + csp[3]);
  SB();
  STAMP(1);
  BAR();
  STAMP(2);
  const int hh = lane >> 5, blk = (lane >> 4) & 1, q = (lane & 15) >> 2, p = lane & 3;
  const int sw = (q >> 1) & 1;
  const LAS char* b0 = smem + M_HT + (8 * hh + q) * 128 + ((2 * blk + (p >> 1) + 4 * sw) * 16) + (p & 1) * 8;
  const LAS char* b1 = smem + M_HT + (8 * hh + q) * 128 + ((2 * blk + (p >> 1) + 4 * (1 - sw)) * 16) + (p & 1) * 8;
  f32x16 acc[2][2];
#pragma unroll
  for (int a = 0; a < 2; ++a)
#pragma unroll
    for (int c = 0; c < 2; ++c)
#pragma unroll
      for (int e = 0; e < 16; ++e) acc[a][c][e] = 0.f;
  float s0 = 0.f, s1 = 0.f, q0 = 0.f, q1 = 0.f;
  const unsigned ONES = 0x3f803f80u;
  constexpr int BD = 3;
  u32x4 bf[BD + 1][2];
  auto bload = [&](int i, u32x4& o0, u32x4& o1) {
    const int go = ((((i / 7) + w) & 3) * 7 + (i % 7)) * 2048;
    o0 = tr_read2(b0 + go, b0 + go + 512);
    o1 = tr_read2(b1 + go, b1 + go + 512);
  };
  float mr[2], ri[2];
  u32x4 own[2][4];
  auto epi = [&](int a, int c, int s) {
    u32x4 fr;
    const f32x4 c0 = *(const LAS f32x4*)(smem + M_CS + (w * 64 + a * 32 + 16 * s + 4 * hh) * 4);
    const f32x4 c1 = *(const LAS f32x4*)(smem + M_CS + (w * 64 + a * 32 + 16 * s + 8 + 4 * hh) * 4);
#pragma unroll
    for (int e = 0; e < 4; ++e) {
      const int r0 = 8 * s + 2 * e;
      const float k0 = e < 2 ? c0[2 * e] : c1[2 * e - 4], k1 = e < 2 ? c0[2 * e + 1] : c1[2 * e - 3];
      const float v0 = (acc[a][c][r0] - mr[c] * k0) * ri[c], v1 = (acc[a][c][r0 + 1] - mr[c] * k1) * ri[c];
      fr[e] = pkbf(fmaxf(v0, 0.f), fmaxf(v1, 0.f));
    }
    own[c][2 * a + s] = fr;
    const int fs = 16 * j + 4 * w + 2 * a + s;
    __builtin_amdgcn_raw_buffer_store_b128(fr, rH1, l16, ((rt * 2 + c) * 112 + fs) * 1024, 16);
  };
#pragma unroll
  for (int i = 0; i < BD; ++i) bload(i, bf[i][0], bf[i][1]);
  static_for<0, 56>([&](auto T_) {
    constexpr int t = decltype(T_)::value;
    constexpr int a = t / 28;
    if constexpr (t + BD < 56) bload((t + BD) % 28, bf[(t + BD) % (BD + 1)][0], bf[(t + BD) % (BD + 1)][1]);
    const u32x4 f0 = bf[t % (BD + 1)][0], f1 = bf[t % (BD + 1)][1];
    static_for<win_tgt(t - 1), win_tgt(t)>([&](auto N_) { seqload(decltype(N_)::value); });
    SB();
    acc[a][0] = mfma32b(wa[t], f0, acc[a][0]);
    acc[a][1] = mfma32b(wa[t], f1, acc[a][1]);
    if constexpr (t < 7) {
#pragma unroll
      for (int e = 0; e < 4; ++e) { s0 = dot2bf(f0[e], ONES, s0); q0 = dot2bf(f0[e], f0[e], q0); s1 = dot2bf(f1[e], ONES, s1); q1 = dot2bf(f1[e], f1[e], q1); }
    }
    if constexpr (t == 8) {
      s0 += __shfl_xor(s0, 32); s1 += __shfl_xor(s1, 32); q0 += __shfl_xor(q0, 32); q1 += __shfl_xor(q1, 32);
      if (hh == 0) {
        *(LAS u32x2*)(smem + M_ST + (w * 64 + lane) * 8) = u32x2{__float_as_uint(s0), __float_as_uint(q0)};
        *(LAS u32x2*)(smem + M_ST + (w * 64 + 32 + lane) * 8) = u32x2{__float_as_uint(s1), __float_as_uint(q1)};
      }
    }
    if constexpr (t == 27) {
      STAMP(3);
      BAR();
      STAMP(4);
#pragma unroll
      for (int c = 0; c < 2; ++c) {
        float s = 0.f, qq = 0.f;
#pragma unroll
        for (int ww = 0; ww < 4; ++ww) { u32x2 v = *(const LAS u32x2*)(smem + M_ST + (ww * 64 + c * 32 + (lane & 31)) * 8); s += __uint_as_float(v[0]); qq += __uint_as_float(v[1]); }
        const float mean = s * (1.0f / D);
        const float var = fmaxf(qq * (1.0f / D) - mean * mean, 0.f);
        float mb, sb; ln_scalars(mean, var, mb, sb);
        mr[c] = mb; ri[c] = rcp_(sb);
      }
    }
    if constexpr (t >= 30 && t < 54 && (t - 30) % 6 == 0) { constexpr int e = (t - 30) / 6; epi(0, e >> 1, e & 1); }
    SB();
  });
#pragma unroll
  for (int e = 0; e < 4; ++e) { epi(1, e >> 1, e & 1); SB(); }
#pragma unroll
  for (int n = 68; n < 84; ++n) seqload(n);
  SB();
  asm volatile("s_waitcnt vmcnt(16)" ::: "memory");
  GAS u64* flg = (GAS u64*)(ws + WS_FLAGB) + (rt * 4 + w) * 8;
  if (lane == 0) __hip_atomic_store(flg + j, tag, __ATOMIC_RELAXED, __HIP_MEMORY_SCOPE_AGENT);
  SB();
#pragma unroll
  for (int n = 84; n < 102; ++n) seqload(n);
  SB();
  const int fl = lane < 7 ? lane : 0;
  u64 fv = __hip_atomic_load(flg + fl, __ATOMIC_RELAXED, __HIP_MEMORY_SCOPE_AGENT);
  SB();
#pragma unroll
  for (int n = 102; n < 112; ++n) seqload(n);
  SB();
  STAMP(5);
#pragma unroll
  for (int a = 0; a < 2; ++a)
#pragma unroll
    for (int c = 0; c < 2; ++c)
#pragma unroll
      for (int e = 0; e < 16; ++e) acc[a][c][e] = 0.f;
#pragma unroll
  for (int i = 0; i < 4; ++i) {
    acc[0][0] = mfma32b(wb[0][i], own[0][i], acc[0][0]);
    acc[0][1] = mfma32b(wb[0][i], own[1][i], acc[0][1]);
    acc[1][0] = mfma32b(wb[1][i], own[0][i], acc[1][0]);
    acc[1][1] = mfma32b(wb[1][i], own[1][i], acc[1][1]);
  }
  SB();
  {
    for (unsigned spins = 0;; ++spins) {
      if (__all(fv == tag)) break;
      if (spins > kSpinCap) { if (lane == 0) __hip_atomic_store((GAS unsigned*)(ws + WS_TMO), 2u, __ATOMIC_RELAXED, __HIP_MEMORY_SCOPE_AGENT); break; }
      fv = __hip_atomic_load(flg + fl, __ATOMIC_RELAXED, __HIP_MEMORY_SCOPE_AGENT);
    }
  }
  asm volatile("" ::: "memory");
  STAMP(6);
  u32x4 hf[2][24];
  auto h1load = [&](int i) {
    const int n = i >> 2, fsub = i & 3;
    int jj = j + n; jj = jj >= 7 ? jj - 7 : jj;
    const int fs = 16 * jj + 4 * w + fsub;
    hf[0][i - 4] = __builtin_amdgcn_raw_buffer_load_b128(rH1, l16, ((rt * 2) * 112 + fs) * 1024, 16);
    hf[1][i - 4] = __builtin_amdgcn_raw_buffer_load_b128(rH1, l16, ((rt * 2 + 1) * 112 + fs) * 1024, 16);
  };
  constexpr int HW = 10;
#pragma unroll
  for (int i = 4; i < 4 + HW; ++i) h1load(i);
  SB();
  static_for<4, 28>([&](auto I_) {
    constexpr int i = decltype(I_)::value;
    if constexpr (i + HW < 28) h1load(i + HW);
    SB();
    acc[0][0] = mfma32b(wb[0][i], hf[0][i - 4], acc[0][0]);
    acc[0][1] = mfma32b(wb[0][i], hf[1][i - 4], acc[0][1]);
    acc[1][0] = mfma32b(wb[1][i], hf[0][i - 4], acc[1][0]);
    acc[1][1] = mfma32b(wb[1][i], hf[1][i - 4], acc[1][1]);
    SB();
  });
  STAMP(7);
#pragma unroll
  for (int a = 0; a < 2; ++a)
#pragma unroll
    for (int c = 0; c < 2; ++c)
#pragma unroll
      for (int g4 = 0; g4 < 4; ++g4) {
        f32x4 v = {acc[a][c][4 * g4], acc[a][c][4 * g4 + 1], acc[a][c][4 * g4 + 2], acc[a][c][4 * g4 + 3]};
        *(LAS f32x4*)(smem + M_P + (w * 64 + c * 32 + (lane & 31)) * 272 + (a * 32 + 8 * g4 + 4 * hh) * 4) = v;
      }
  BAR();
  STAMP(8);
  {
    const int l = lane, c4 = w;
    float o[16];
#pragma unroll
    for (int e = 0; e < 16; ++e) o[e] = 0.f;
#pragma unroll
    for (int ww = 0; ww < 4; ++ww)
#pragma unroll
      for (int i = 0; i < 4; ++i) {
        f32x4 v = *(const LAS f32x4*)(smem + M_P + (ww * 64 + l) * 272 + (16 * c4 + 4 * i) * 4);
#pragma unroll
        for (int e = 0; e < 4; ++e) o[4 * i + e] += v[e];
      }
    unsigned pk[8];
#pragma unroll
    for (int e = 0; e < 8; ++e) {
      const int r0 = 64 * j + 16 * c4 + 2 * e, r1 = r0 + 1;
      const float h0 = bf2f(*(const LAS unsigned short*)(smem + M_HT + r0 * 128 + (((l >> 3) ^ (4 * ((r0 >> 1) & 1))) * 16) + (l & 7) * 2));
      const float h1 = bf2f(*(const LAS unsigned short*)(smem + M_HT + r1 * 128 + (((l >> 3) ^ (4 * ((r1 >> 1) & 1))) * 16) + (l & 7) * 2));
      const unsigned ob = pkbf(o[2 * e], o[2 * e + 1]);
      pk[e] = pkbf(h0 + bflo(ob), h1 + bfhi(ob));
    }
    char* op = (char*)out + ((size_t)(rt * 64 + l) * D + 64 * j + 16 * c4) * 2;
    *(u32x4*)op = u32x4{pk[0], pk[1], pk[2], pk[3]};
    *(u32x4*)(op + 16) = u32x4{pk[4], pk[5], pk[6], pk[7]};
  }
  STAMP(9);
  STAMP_FLUSH(1);
}

}

extern "C" void kernel_launch(void* const* d_in, const int* in_sizes, int n_in, void* d_out, int out_size, void* d_ws, size_t ws_size, hipStream_t stream) {
  typedef const unsigned short* cp;
  k_ssm<<<256, 512, 0, stream>>>((cp)d_in[0], (cp)d_in[1], (cp)d_in[2], (cp)d_in[3], (cp)d_in[4], (cp)d_in[5], (cp)d_in[6], (cp)d_in[7], (cp)d_in[8], (cp)d_in[10], (cp)d_in[11], (char*)d_ws);
  k_mlp<<<224, 256, 0, stream>>>((char*)d_ws, (unsigned short*)d_out);
}
```

```cpp
#include <hip/hip_runtime.h>
#include <stdint.h>

namespace {

typedef __attribute__((ext_vector_type(8))) _Float16 f16x8;
typedef __attribute__((ext_vector_type(4))) _Float16 f16x4;
typedef __attribute__((ext_vector_type(2))) _Float16 f16x2;
typedef __attribute__((ext_vector_type(8))) __bf16 bf16x8;
typedef __attribute__((ext_vector_type(2))) __bf16 bf16x2;
typedef __attribute__((ext_vector_type(4))) float f32x4;
typedef __attribute__((ext_vector_type(16))) float f32x16;
typedef __attribute__((ext_vector_type(4))) unsigned u32x4;
typedef __attribute__((ext_vector_type(2))) unsigned u32x2;
typedef __attribute__((ext_vector_type(4))) short s16x4;
typedef unsigned long long u64;
#define LAS __attribute__((address_space(3)))
#define GAS __attribute__((address_space(1)))
#define DI __device__ __forceinline__

constexpr int L = 2048, D = 448, NM = 256, F = 1792;
#define CW_A 12
#define CW_M 12

constexpr size_t WS_EPOCH = 0;
constexpr size_t WS_TMO = 64;
constexpr size_t WS_FLAGB = 8192;
constexpr size_t WS_STATS = 16384;
constexpr size_t WS_XT = WS_STATS + 1024 * 16;
constexpr size_t WS_CS = WS_XT + (size_t)512 * D * 16;
constexpr size_t WS_HT = WS_CS + 4 * F * 4;
constexpr size_t WS_W1F = WS_HT + (size_t)D * L * 2;
constexpr size_t WS_W2F = WS_W1F + (size_t)D * F * 2;
constexpr size_t WS_H1F = WS_W2F + (size_t)D * F * 2;
constexpr size_t WS_END = WS_H1F + (size_t)L * F * 2;
constexpr size_t WS_STAMP = WS_END;
constexpr unsigned kSpinCap = 1u << 20;

#define STAMP(i) do {} while (0)
#define STAMP_DECL
#define STAMP_FLUSH(k) do {} while (0)
#define SB() __builtin_amdgcn_sched_barrier(0)
#define BAR() do { asm volatile("s_waitcnt lgkmcnt(0)" ::: "memory"); __builtin_amdgcn_s_barrier(); asm volatile("" ::: "memory"); } while (0)
DI float bf2f(unsigned b) { return __uint_as_float(b << 16); }
DI float bflo(unsigned p) { return __uint_as_float(p << 16); }
DI float bfhi(unsigned p) { return __uint_as_float(p & 0xffff0000u); }
DI unsigned pkbf(float a, float b) { bf16x2 r; r[0] = (__bf16)a; r[1] = (__bf16)b; return __builtin_bit_cast(unsigned, r); }
DI float rbf(float a) { return bflo(pkbf(a, a)); }
DI unsigned pkh(float a, float b) { f16x2 r; r[0] = (_Float16)a; r[1] = (_Float16)b; return __builtin_bit_cast(unsigned, r); }

template <int CTRL> DI float dppf(float v) { return __int_as_float(__builtin_amdgcn_update_dpp(0, __float_as_int(v), CTRL, 0xF, 0xF, true)); }
DI float rdl(float v, int l) { return __int_as_float(__builtin_amdgcn_readlane(__float_as_int(v), l)); }
DI float wave_sum(float v) {
  v += dppf<0xB1>(v); v += dppf<0x4E>(v); v += dppf<0x141>(v); v += dppf<0x140>(v);
  return (rdl(v, 0) + rdl(v, 16)) + (rdl(v, 32) + rdl(v, 48));
}
DI float wave_max(float v) {
  v = fmaxf(v, dppf<0xB1>(v)); v = fmaxf(v, dppf<0x4E>(v)); v = fmaxf(v, dppf<0x141>(v)); v = fmaxf(v, dppf<0x140>(v));
  return fmaxf(fmaxf(rdl(v, 0), rdl(v, 16)), fmaxf(rdl(v, 32), rdl(v, 48)));
}
DI s16x4 tr_read(const LAS char* p) { return __builtin_amdgcn_ds_read_tr16_b64_v4i16((LAS s16x4*)p); }
DI u32x4 tr_read2(const LAS char* p0, const LAS char* p1) {
  s16x4 a = tr_read(p0), b = tr_read(p1);
  u32x2 a2 = __builtin_bit_cast(u32x2, a), b2 = __builtin_bit_cast(u32x2, b);
  u32x4 r; r[0] = a2[0]; r[1] = a2[1]; r[2] = b2[0]; r[3] = b2[1]; return r;
}
DI f32x4 mfma16h(u32x4 a, u32x4 b, f32x4 c) { return __builtin_amdgcn_mfma_f32_16x16x32_f16(__builtin_bit_cast(f16x8, a), __builtin_bit_cast(f16x8, b), c, 0, 0, 0); }
DI f32x16 mfma32b(u32x4 a, u32x4 b, f32x16 c) { return __builtin_amdgcn_mfma_f32_32x32x16_bf16(__builtin_bit_cast(bf16x8, a), __builtin_bit_cast(bf16x8, b), c, 0, 0, 0); }
DI float dot2bf(unsigned a, unsigned b, float c) { return __builtin_amdgcn_fdot2_f32_bf16(__builtin_bit_cast(bf16x2, a), __builtin_bit_cast(bf16x2, b), c, false); }

DI u64 load_epoch(const char* ws) {
  const u32x2 v = *(const u32x2*)(ws + WS_EPOCH);
  unsigned lo = __builtin_amdgcn_readfirstlane(v[0]), hi = __builtin_amdgcn_readfirstlane(v[1]);
  asm volatile("" : "+s"(lo), "+s"(hi));
  return ((u64)hi << 32) | lo;
}
template <int I> struct IC { static constexpr int value = I; };
template <int B, int E, class Fn> DI void static_for(Fn&& fn) { if constexpr (B < E) { fn(IC<B>{}); static_for<B + 1, E>(fn); } }
template <int NKB> DI void code_warm_issue(u32x4 (&v)[NKB], int lane, bool on) {
  const char* pc = (const char*)(__builtin_amdgcn_s_getpc() & ~(unsigned long long)63);
#pragma unroll
  for (int i = 0; i < NKB; ++i) v[i] = u32x4{0u, 0u, 0u, 0u};
  if (on) {
#pragma unroll
    for (int i = 0; i < NKB; ++i) v[i] = *(const u32x4*)(pc + i * 1024 + lane * 16);
  }
}
template <int NKB> DI void code_warm_retire(u32x4 (&v)[NKB]) {
#pragma unroll
  for (int i = 0; i < NKB; ++i) asm volatile("" :: "v"(v[i]));
}
struct cf { float r, i; };
DI cf cmul(cf a, cf b) { cf o; o.r = a.r * b.r - a.i * b.i; o.i = a.r * b.i + a.i * b.r; return o; }
DI cf csq(cf a) { cf o; o.r = a.r * a.r - a.i * a.i; o.i = 2.f * a.r * a.i; return o; }
DI cf cexp_p2(float zr2, float zf, float p2) {
  float m = __builtin_amdgcn_exp2f(zr2 * p2);
  float t = __builtin_amdgcn_fractf(zf * p2);
  cf o; o.r = m * __builtin_amdgcn_cosf(t); o.i = m * __builtin_amdgcn_sinf(t); return o;
}

DI void ln_scalars(float mean, float var, float& mb, float& sb) {
  mb = rbf(mean);
  float vb = rbf(var);
  sb = rbf(__builtin_sqrtf(rbf(vb + 1e-5f)));
}
DI float rcp_(float s) { return __builtin_amdgcn_rcpf(s); }

struct IdxTab { unsigned short v[256][12]; };
constexpr int bf16_round_index(int lag) {
  if (lag < 0 || lag > 2047) return 641;
  if (lag < 256) return lag;
  int e = 8; while ((lag >> (e + 1)) != 0) ++e;
  const int sh = e - 7;
  int q = lag >> sh; const int r = lag & ((1 << sh) - 1), half = 1 << (sh - 1);
  if (r > half || (r == half && (q & 1))) ++q;
  return 256 + (e - 8) * 128 + (q - 128);
}
constexpr IdxTab make_idx() {
  IdxTab t{};
  for (int c = 0; c < 256; ++c) { for (int e = 0; e < 11; ++e) t.v[c][e] = (unsigned short)(2 * bf16_round_index(2048 - 8 * c + 3 - e)); t.v[c][11] = 0; }
  return t;
}
__device__ const IdxTab g_idx = make_idx();

constexpr int A_TAB = 0;
constexpr int A_MISC = A_TAB + 8 * 16384;
constexpr int A_KDC = A_MISC;
constexpr int A_SC = A_KDC + 2 * 1312;
constexpr int A_XS = A_SC + 128;
constexpr int A_LDS = A_XS + 9216 + 64;
constexpr int TOE_CP = 4160, TOE_CH = 4 * TOE_CP;
constexpr int A_U16 = A_TAB + 4096;
constexpr int A_TOE2 = A_TAB + 13312;
constexpr int A_YP = A_TAB + 46592;

__global__ __launch_bounds__(512, 2) void k_ssm(const unsigned short* __restrict__ x, const unsigned short* __restrict__ Are, const unsigned short* __restrict__ Aim,
                                                 const unsigned short* __restrict__ Bre, const unsigned short* __restrict__ Bim, const unsigned short* __restrict__ Cre,
                                                 const unsigned short* __restrict__ Cim, const unsigned short* __restrict__ logdt, const unsigned short* __restrict__ Dsk,
                                                 const unsigned short* __restrict__ w1, const unsigned short* __restrict__ w2, char* __restrict__ ws, const unsigned short* __restrict__ out_) {
  __shared__ __attribute__((aligned(16))) char smem_[A_LDS];
  LAS char* smem = (LAS char*)smem_;
  const int tid = threadIdx.x, lane = tid & 63;
  const int w = __builtin_amdgcn_readfirstlane(tid >> 6);
  const int b = blockIdx.x;
  const bool has_ch = b < 224;
  const int c2 = w >> 2, w4 = w & 3;
  const int ch = 2 * b + c2;
  STAMP_DECL
  STAMP(0);
  u32x4 cw[CW_A];
  code_warm_issue<CW_A>(cw, lane, w == 7);
  const int bs = has_ch ? b : 0;
  const int chs = 2 * bs + c2;
  const int po = chs * NM + w4 * 64 + lane;
  const unsigned short r_are = Are[po], r_aim = Aim[po], r_bre = Bre[po], r_bim = Bim[po], r_cre = Cre[po], r_cim = Cim[po];
  const unsigned short r_ldt = logdt[chs];
  const u32x2 ev = *(const u32x2*)(ws + WS_EPOCH);
  SB();
  const int row = 8 * b + w;
  u32x4 xv = *(const u32x4*)(x + (size_t)row * D + min(lane, 55) * 8);
  const int trt = bs & 31, tg = bs >> 5;
  const u32x4 xt = *(const u32x4*)(x + (size_t)(64 * trt + 8 * w + (lane >> 3)) * D + 64 * tg + (lane & 7) * 8);
  const unsigned r_dsk = *(const unsigned*)(Dsk + 2 * bs);
  const int t = tid & 255;
  const u32x4 ix0 = *(const u32x4*)&g_idx.v[t][0];
  const u32x2 ix1 = *(const u32x2*)&g_idx.v[t][8];
  SB();
  unsigned elo = __builtin_amdgcn_readfirstlane(ev[0]), ehi = __builtin_amdgcn_readfirstlane(ev[1]);
  asm volatile("" : "+s"(elo), "+s"(ehi));
  const u64 tag = (((u64)ehi << 32) | elo) + 1;
  const unsigned tlo_ = (unsigned)tag, thi_ = (unsigned)(tag >> 32);
  LAS char* tab = smem + A_TAB + w * 16384;

  auto ln_row = [&]() {
    if (lane >= 56) xv = u32x4{0u, 0u, 0u, 0u};
    float xf[8];
#pragma unroll
    for (int j = 0; j < 4; ++j) { xf[2 * j] = bflo(xv[j]); xf[2 * j + 1] = bfhi(xv[j]); }
    float s = 0.f;
#pragma unroll
    for (int j = 0; j < 8; ++j) s += xf[j];
    const float mean = wave_sum(s) * (1.0f / D);
    float q = 0.f;
#pragma unroll
    for (int j = 0; j < 8; ++j) { float d = xf[j] - mean; q += d * d; }
    if (lane >= 56) q = 0.f;
    const float var = wave_sum(q) * (1.0f / D);
    float mb, sb; ln_scalars(mean, var, mb, sb);
    if (lane == 0) *(LAS unsigned*)(smem + A_XS + 9216 + w * 4) = (__float_as_uint(mb) >> 16) | (__float_as_uint(sb) & 0xffff0000u);
    if (has_ch) *(LAS u32x4*)(smem + A_XS + (8 * w + (lane >> 3)) * 144 + (lane & 7) * 16) = xt;
  };
  auto ux_publish = [&]() {
    if (tid < 4) {
      const u32x2 p = *(const LAS u32x2*)(smem + A_XS + 9216 + tid * 8);
      auto rs = __builtin_amdgcn_make_buffer_rsrc((void*)(ws + WS_STATS), 0, 1024 * 16, 0x00020000);
      __builtin_amdgcn_raw_buffer_store_b128(u32x4{p[0], p[1], tlo_, thi_}, rs, (4 * b + tid) * 16, 0, 16);
    }
    if (has_ch) {
      auto rx = __builtin_amdgcn_make_buffer_rsrc((void*)(ws + WS_XT), 0, 512 * D * 16, 0x00020000);
      const int qd = lane & 15;
#pragma unroll
      for (int i = 0; i < 2; ++i) {
        const int c = 8 * w + 4 * i + (lane >> 4);
        const LAS char* a0 = smem + A_XS + (4 * qd) * 144 + c * 2;
        const unsigned r0 = *(const LAS unsigned short*)a0, r1 = *(const LAS unsigned short*)(a0 + 144), r2 = *(const LAS unsigned short*)(a0 + 288), r3 = *(const LAS unsigned short*)(a0 + 432);
        __builtin_amdgcn_raw_buffer_store_b128(u32x4{r0 | (r1 << 16), r2 | (r3 << 16), tlo_, thi_}, rx, ((64 * tg + c) * 512 + 16 * trt + qd) * 16, 0, 16);
      }
    }
  };
  STAMP(1);

  if (!has_ch) {
    code_warm_retire<CW_A>(cw);
    ln_row();
    BAR();
    ux_publish();
    const int task = (b - 224) * 8 + w;
    if (task < 224) {
      const bool is_w1 = task < 112;
      const int tk = is_w1 ? task : task - 112;
      const int nb = is_w1 ? tk % 28 : tk % 7, kg = is_w1 ? tk / 28 : tk / 7;
      const int rld = is_w1 ? F : D;
      const unsigned short* src = (is_w1 ? w1 : w2) + (size_t)(16 * 7 * kg + (lane >> 3)) * rld + 64 * nb + (lane & 7) * 8;
      u32x4 r0[7], r1[7];
#pragma unroll
      for (int u = 0; u < 7; ++u) { r0[u] = *(const u32x4*)(src + (size_t)(16 * u) * rld); r1[u] = *(const u32x4*)(src + (size_t)(16 * u + 8) * rld); }
      LAS char* sc = smem + w * 16384;
#pragma unroll
      for (int u = 0; u < 7; ++u) { *(LAS u32x4*)(sc + u * 2048 + lane * 16) = r0[u]; *(LAS u32x4*)(sc + u * 2048 + 1024 + lane * 16) = r1[u]; }
      __builtin_amdgcn_s_waitcnt(0xC07F);
      __builtin_amdgcn_wave_barrier();
      const int g = lane >> 4, blk = g & 1, hh = g >> 1, q = (lane & 15) >> 2, p = lane & 3;
      const int q0 = is_w1 ? (8 * hh + q) : (4 * hh + q);
      const int q1 = is_w1 ? (8 * hh + 4 + q) : (8 + 4 * hh + q);
      auto rd = __builtin_amdgcn_make_buffer_rsrc((void*)(ws + (is_w1 ? WS_W1F : WS_W2F)), 0, D * F * 2, 0x00020000);
      const int nks = is_w1 ? 28 : 112;
      float cs[2] = {0.f, 0.f};
#pragma unroll
      for (int u = 0; u < 7; ++u) {
#pragma unroll
        for (int tt = 0; tt < 2; ++tt) {
          const int cb = (tt * 32 + blk * 16 + 4 * p) * 2;
          u32x4 fr = tr_read2(sc + u * 2048 + q0 * 128 + cb, sc + u * 2048 + q1 * 128 + cb);
          __builtin_amdgcn_raw_buffer_store_b128(fr, rd, lane * 16, ((2 * nb + tt) * nks + 7 * kg + u) * 1024, 16);
#pragma unroll
          for (int e = 0; e < 4; ++e) cs[tt] = dot2bf(fr[e], 0x3f803f80u, cs[tt]);
        }
      }
      if (is_w1) {
        cs[0] += __shfl_xor(cs[0], 32); cs[1] += __shfl_xor(cs[1], 32);
        auto rc = __builtin_amdgcn_make_buffer_rsrc((void*)(ws + WS_CS), 0, 4 * F * 4, 0x00020000);
        __builtin_amdgcn_raw_buffer_store_b32(__float_as_uint(lane < 32 ? cs[0] : cs[1]), rc, (kg * F + 64 * nb + lane) * 4, 0, 16);
      }
    }
    if (b < 232) {
      const int xr = (b & 7) * 4;
      unsigned v = 0;
      if (tid < 224) v = *(const volatile unsigned*)(ws + WS_H1F + (size_t)xr * 229376 + (size_t)tid * 4096);
      else if (tid < 280) v = *(const volatile unsigned*)((const char*)out_ + (size_t)xr * 57344 + (size_t)(tid - 224) * 4096);
      asm volatile("" :: "v"(v));
    }
    return;
  }

  STAMP(13);
  float k2048;
  int ew;
  {
    const float are = bf2f(r_are), aim = bf2f(r_aim), bre = bf2f(r_bre), bim = bf2f(r_bim), cre = bf2f(r_cre), cim = bf2f(r_cim);
    const float dt = rbf(expf(bf2f(r_ldt)));
    const float zr = dt * are, zi = dt * aim;
    const float zr2 = zr * 1.44269504088896341f, zf = zi * 0.15915494309189535f;
    const float dbr = dt * bre, dbi = dt * bim;
    cf CB; CB.r = cre * dbr - cim * dbi; CB.i = cre * dbi + cim * dbr;
    const float mx = wave_max(fmaxf(fabsf(CB.r), fabsf(CB.i)));
    const float gz = fmaxf(wave_max(zr2), 0.f) * 2048.f;
    int e = (mx > 0.f) ? ((int)(__float_as_uint(mx) >> 23) - 127) : 0;
    e += (int)ceilf(fminf(gz, 100.f));
    e = max(-120, min(120, e));
    ew = e;
    const float S = __uint_as_float((unsigned)(127 - e) << 23);
    cf G0; G0.r = CB.r * S; G0.i = CB.i * S;
    const cf E1 = cexp_p2(zr2, zf, 1.f), E2 = csq(E1), E4 = cexp_p2(zr2, zf, 4.f), E8 = csq(E4);
    const cf E16 = cexp_p2(zr2, zf, 16.f), E32 = csq(E16), E64 = cexp_p2(zr2, zf, 64.f), E128 = csq(E64);
    const cf E256 = cexp_p2(zr2, zf, 256.f), E512 = csq(E256), E1024 = cexp_p2(zr2, zf, 1024.f), E2048 = csq(E1024);
    STAMP(14);
    SB();
    ln_row();
    SB();
    const int sw = (lane >> 1) & 3;
    LAS char* tb = tab + lane * 64;
    auto put = [&](int tile, int half, const cf* v, float isign) {
      u32x4 re, im;
#pragma unroll
      for (int j = 0; j < 4; ++j) { re[j] = pkh(v[2 * j].r, v[2 * j + 1].r); im[j] = pkh(isign * v[2 * j].i, isign * v[2 * j + 1].i); }
      *(LAS u32x4*)(tb + tile * 4096 + ((half ^ sw) * 16)) = re;
      *(LAS u32x4*)(tb + tile * 4096 + (((2 + half) ^ sw) * 16)) = im;
    };
    cf Er[8];
    Er[0].r = 1.f; Er[0].i = 0.f; Er[1] = E1; Er[2] = E2; Er[3] = cmul(E1, E2); Er[4] = E4; Er[5] = cmul(E4, E1); Er[6] = cmul(E4, E2); Er[7] = cmul(E4, Er[3]);
    {
      cf Y[8];
      Y[0].r = 1.f; Y[0].i = 0.f; Y[1] = E8; Y[2] = E16; Y[3] = cmul(E8, E16); Y[4] = E32; Y[5] = cmul(E32, E8); Y[6] = cmul(E32, E16); Y[7] = cmul(E32, Y[3]);
      put(3, 0, Y, 1.f);
      cf Y2[8];
      Y2[0] = E64;
#pragma unroll
      for (int j = 1; j < 8; ++j) Y2[j] = cmul(Y[j], E64);
      put(3, 1, Y2, 1.f);
    }
    STAMP(15);
    cf G[8];
    G[0] = G0; G[1] = cmul(G0, E128); G[2] = cmul(G0, E256); G[3] = cmul(G[1], E256);
#pragma unroll
    for (int j = 0; j < 4; ++j) G[4 + j] = cmul(G[j], E512);
    {
      cf T[8];
      T[0] = G[0];
#pragma unroll
      for (int j = 1; j < 8; ++j) T[j] = cmul(G[0], Er[j]);
      put(0, 0, T, -1.f);
      SB();
      BAR();
      ux_publish();
      SB();
      T[0] = G[1];
#pragma unroll
      for (int j = 1; j < 8; ++j) T[j] = cmul(G[1], Er[j]);
      put(0, 1, T, -1.f);
    }
    {
      cf T[8];
      T[0] = G[2]; T[1] = cmul(G[2], E2); T[2] = cmul(G[2], E4); T[3] = cmul(G[2], Er[6]);
      T[4] = G[3]; T[5] = cmul(G[3], E2); T[6] = cmul(G[3], E4); T[7] = cmul(G[3], Er[6]);
      put(1, 0, T, -1.f);
#pragma unroll
      for (int j = 0; j < 4; ++j) { T[2 * j] = G[4 + j]; T[2 * j + 1] = cmul(G[4 + j], E4); }
      put(1, 1, T, -1.f);
    }
    {
      cf T[8];
#pragma unroll
      for (int j = 0; j < 8; ++j) T[j] = cmul(G[j], E1024);
      put(2, 0, T, -1.f);
    }
    k2048 = wave_sum(G0.r * E2048.r - G0.i * E2048.i);
  }
  STAMP(2);
  __builtin_amdgcn_s_waitcnt(0xC07F);
  __builtin_amdgcn_wave_barrier();
  {
    const int g = lane >> 4, q = (lane & 15) >> 2, p = lane & 3;
    f32x4 acc0 = {0.f, 0.f, 0.f, 0.f}, acc1 = acc0, acc2 = acc0;
    const int part = q & 1, cl = part * 2 + (p >> 1);
    u32x4 bb[4], a0[4], a1[4], a2[4];
#pragma unroll
    for (int s = 0; s < 4; ++s) {
      const int m0 = 16 * s + 8 * (g >> 1) + 2 * (g & 1) + (q >> 1), m1 = m0 + 4;
      const int o0 = m0 * 64 + ((cl ^ ((m0 >> 1) & 3)) * 16) + (p & 1) * 8;
      const int o1 = m1 * 64 + ((cl ^ ((m1 >> 1) & 3)) * 16) + (p & 1) * 8;
      bb[s] = tr_read2(tab + 3 * 4096 + o0, tab + 3 * 4096 + o1);
      a0[s] = tr_read2(tab + o0, tab + o1);
      a1[s] = tr_read2(tab + 4096 + o0, tab + 4096 + o1);
      a2[s] = tr_read2(tab + 8192 + o0, tab + 8192 + o1);
    }
    SB();
#pragma unroll
    for (int s = 0; s < 4; ++s) { acc0 = mfma16h(a0[s], bb[s], acc0); acc1 = mfma16h(a1[s], bb[s], acc1); acc2 = mfma16h(a2[s], bb[s], acc2); }
    SB();
    *(LAS f32x4*)(tab + lane * 16) = acc0;
    *(LAS f32x4*)(tab + 1024 + lane * 16) = acc1;
    *(LAS f32x4*)(tab + 2048 + lane * 16) = acc2;
    if (lane == 0) { *(LAS int*)(smem + A_SC + w * 8) = ew; *(LAS float*)(smem + A_SC + w * 8 + 4) = k2048; }
  }
  STAMP(3);
  auto rst = __builtin_amdgcn_make_buffer_rsrc((void*)(ws + WS_STATS), 0, 1024 * 16, 0x00020000);
  auto rxq = __builtin_amdgcn_make_buffer_rsrc((void*)(ws + WS_XT), 0, 512 * D * 16, 0x00020000);
  u32x4 gr[4];
  auto sweep = [&]() {
    gr[0] = __builtin_amdgcn_raw_buffer_load_b128(rst, tid * 32, 0, 16);
    gr[1] = __builtin_amdgcn_raw_buffer_load_b128(rst, tid * 32 + 16, 0, 16);
    gr[2] = __builtin_amdgcn_raw_buffer_load_b128(rxq, tid * 16, (2 * b) * 8192, 16);
    gr[3] = __builtin_amdgcn_raw_buffer_load_b128(rxq, tid * 16, (2 * b + 1) * 8192, 16);
  };
  sweep();
  BAR();
  STAMP(4);
  float SKinv;
  {
    int e4[4]; float k4[4];
#pragma unroll
    for (int j = 0; j < 4; ++j) { e4[j] = *(LAS int*)(smem + A_SC + (c2 * 4 + j) * 8); k4[j] = *(LAS float*)(smem + A_SC + (c2 * 4 + j) * 8 + 4); }
    const int em = max(max(e4[0], e4[1]), max(e4[2], e4[3]));
    float fw[4];
#pragma unroll
    for (int j = 0; j < 4; ++j) { int d = max(e4[j] - em + 5, -120); fw[j] = __uint_as_float((unsigned)(127 + d) << 23); }
    SKinv = __uint_as_float((unsigned)(127 + em - 5) << 23);
    LAS char* kdc = smem + A_KDC + c2 * 1312;
    if (t < 192) {
      const int tile = t >> 6, ln = t & 63, c = ln & 15, g = ln >> 4;
      f32x4 s = {0.f, 0.f, 0.f, 0.f};
#pragma unroll
      for (int j = 0; j < 4; ++j) { f32x4 v = *(LAS f32x4*)(smem + A_TAB + (c2 * 4 + j) * 16384 + tile * 1024 + ln * 16); s += v * fw[j]; }
      const unsigned p01 = pkh(s[0], s[1]), p23 = pkh(s[2], s[3]);
      if (tile == 0) {
        u32x2 v = {p01, p23}; *(LAS u32x2*)(kdc + (128 * (g >> 1) + 8 * c + 4 * (g & 1)) * 2) = v;
      } else if (tile == 1) {
        if (g < 2) { u32x2 v = {p01, p23}; *(LAS u32x2*)(kdc + (256 + 64 * g + 4 * c) * 2) = v; }
        else { const int i0 = 384 + 64 * (g - 2) + 2 * c; *(LAS unsigned*)(kdc + i0 * 2) = p01; *(LAS unsigned*)(kdc + (i0 + 32) * 2) = p23; }
      } else if (g < 2) {
        const int i0 = 512 + 64 * g + c;
        *(LAS unsigned short*)(kdc + i0 * 2) = (unsigned short)p01; *(LAS unsigned short*)(kdc + (i0 + 16) * 2) = (unsigned short)(p01 >> 16);
        *(LAS unsigned short*)(kdc + (i0 + 32) * 2) = (unsigned short)p23; *(LAS unsigned short*)(kdc + (i0 + 48) * 2) = (unsigned short)(p23 >> 16);
      }
    } else if (t == 192) {
      const float k = k4[0] * fw[0] + k4[1] * fw[1] + k4[2] * fw[2] + k4[3] * fw[3];
      *(LAS unsigned*)(kdc + 640 * 2) = pkh(k, 0.f);
    }
  }
  float SKo;
  {
    int e4[4];
#pragma unroll
    for (int j = 0; j < 4; ++j) e4[j] = *(LAS int*)(smem + A_SC + ((1 - c2) * 4 + j) * 8);
    const int em = max(max(e4[0], e4[1]), max(e4[2], e4[3]));
    SKo = __uint_as_float((unsigned)(127 + em - 5) << 23);
  }
  const float SK0 = c2 ? SKo : SKinv, SK1 = c2 ? SKinv : SKo;
  unsigned ub[4], xc[4];
  {
    for (unsigned spins = 0;; ++spins) {
      bool ok = true;
#pragma unroll
      for (int i = 0; i < 4; ++i) ok = ok && gr[i][2] == tlo_ && gr[i][3] == thi_;
      if (__all(ok)) break;
      if (spins > kSpinCap) { if (lane == 0) __hip_atomic_store((GAS unsigned*)(ws + WS_TMO), 1u, __ATOMIC_RELAXED, __HIP_MEMORY_SCOPE_AGENT); break; }
      sweep();
    }
    xc[0] = (gr[2][0] & 0xffffu) | (gr[3][0] << 16); xc[1] = (gr[2][0] >> 16) | (gr[3][0] & 0xffff0000u);
    xc[2] = (gr[2][1] & 0xffffu) | (gr[3][1] << 16); xc[3] = (gr[2][1] >> 16) | (gr[3][1] & 0xffff0000u);
    float u0[4], u1[4];
#pragma unroll
    for (int i = 0; i < 4; ++i) {
      const unsigned pay = gr[i >> 1][i & 1];
      const float mb = bflo(pay), ri = rcp_(bfhi(pay));
      const unsigned tt = pkbf(bflo(xc[i]) - mb, bfhi(xc[i]) - mb);
      ub[i] = pkbf(bflo(tt) * ri, bfhi(tt) * ri);
      u0[i] = bflo(ub[i]); u1[i] = bfhi(ub[i]);
    }
    *(LAS u32x2*)(smem + A_U16 + 512 + tid * 8) = u32x2{pkh(u0[0], u0[1]), pkh(u0[2], u0[3])};
    *(LAS u32x2*)(smem + A_U16 + 4608 + 512 + tid * 8) = u32x2{pkh(u1[0], u1[1]), pkh(u1[2], u1[3])};
    if (t < 32) *(LAS u32x4*)(smem + A_U16 + c2 * 4608 + t * 16) = u32x4{0u, 0u, 0u, 0u};
  }
  code_warm_retire<CW_A>(cw);
  STAMP(5);
  BAR();
  u32x4 Bf[2][8];
  {
    const int bq = lane & 15, g = lane >> 4;
    const LAS char* ua = smem + A_U16 + c2 * 4608 + (256 + 16 * (bq - 1) + 8 * g) * 2 - 64 * w4 - 256;
#pragma unroll
    for (int it = 0; it < 2; ++it)
#pragma unroll
      for (int k = 0; k < 8; ++k) Bf[it][k] = *(const LAS u32x4*)(ua + k * 512 + (1 - it) * 256);
  }
  STAMP(6);
  {
    const LAS char* kdc = smem + A_KDC + c2 * 1312;
    LAS char* toe = smem + A_TOE2 + c2 * TOE_CH;
    unsigned v[11];
#pragma unroll
    for (int e = 0; e < 11; ++e) {
      const unsigned wd = e < 8 ? ix0[e >> 1] : ix1[(e - 8) >> 1];
      const unsigned off = (e & 1) ? (wd >> 16) : (wd & 0xffffu);
      v[e] = *(const LAS unsigned short*)(kdc + off);
    }
    unsigned ev_[5], od[5];
#pragma unroll
    for (int j = 0; j < 5; ++j) { ev_[j] = v[2 * j] | (v[2 * j + 1] << 16); od[j] = v[2 * j + 1] | (v[2 * j + 2] << 16); }
    *(LAS u32x4*)(toe + 0 * TOE_CP + t * 16) = u32x4{od[1], od[2], od[3], od[4]};
    *(LAS u32x4*)(toe + 1 * TOE_CP + t * 16) = u32x4{ev_[1], ev_[2], ev_[3], ev_[4]};
    *(LAS u32x4*)(toe + 2 * TOE_CP + t * 16) = u32x4{od[0], od[1], od[2], od[3]};
    *(LAS u32x4*)(toe + 3 * TOE_CP + t * 16) = u32x4{ev_[0], ev_[1], ev_[2], ev_[3]};
    if (t < 12) {
      const int s = t & 3, c = 256 + (t >> 2);
      const u32x2 k03 = *(const LAS u32x2*)kdc;
      const unsigned k0 = k03[0] & 0xffffu, k1 = k03[0] >> 16, k2 = k03[1] & 0xffffu, k3 = k03[1] >> 16;
      unsigned d0 = s == 0 ? k0 : s == 1 ? (k1 | (k0 << 16)) : s == 2 ? (k2 | (k1 << 16)) : (k3 | (k2 << 16));
      unsigned d1 = s == 2 ? k0 : s == 3 ? (k1 | (k0 << 16)) : 0u;
      if (c != 256) { d0 = 0u; d1 = 0u; }
      *(LAS u32x4*)(toe + s * TOE_CP + c * 16) = u32x4{d0, d1, 0u, 0u};
    }
  }
  STAMP(7);
  STAMP(8);
  BAR();
  STAMP(9);
  if (b == 0 && tid == 0) __hip_atomic_store((GAS u64*)(ws + WS_EPOCH), tag, __ATOMIC_RELAXED, __HIP_MEMORY_SCOPE_AGENT);
  {
    const int bq = lane & 15, g = lane >> 4;
    f32x4 acc[8];
#pragma unroll
    for (int i = 0; i < 8; ++i) acc[i] = f32x4{0.f, 0.f, 0.f, 0.f};
    const LAS char* ta = smem + A_TOE2 + c2 * TOE_CH + (bq & 3) * TOE_CP + (2032 - 4 * (bq >> 2) + 8 * g) * 2 - 64 * w4 - 512 * 7 - 256;
    u32x4 A[2][8];
#pragma unroll
    for (int it = 0; it < 2; ++it)
#pragma unroll
      for (int k = 0; k < 8; ++k) {
        const u32x2 lo = *(const volatile LAS u32x2*)(ta + (7 - k) * 512 + (1 - it) * 256), hi = *(const volatile LAS u32x2*)(ta + (7 - k) * 512 + (1 - it) * 256 + 8);
        A[it][k] = u32x4{lo[0], lo[1], hi[0], hi[1]};
      }
    SB();
#pragma unroll
    for (int it = 0; it < 2; ++it) {
#pragma unroll
      for (int k = 0; k < 8; ++k)
#pragma unroll
        for (int i = k; i < 8; ++i) acc[i] = mfma16h(A[it][k], Bf[it][i - k], acc[i]);
      SB();
    }
    LAS char* yp = smem + A_YP + w * 8192;
#pragma unroll
    for (int i = 0; i < 8; ++i) *(LAS f32x4*)(yp + i * 1024 + bq * 64 + g * 16) = acc[i];
  }
  STAMP(10);
  BAR();
  STAMP(11);
  {
    f32x4 y0 = {0.f, 0.f, 0.f, 0.f}, y1 = y0;
#pragma unroll
    for (int j = 0; j < 4; ++j) {
      y0 += *(const LAS f32x4*)(smem + A_YP + j * 8192 + tid * 16);
      y1 += *(const LAS f32x4*)(smem + A_YP + (4 + j) * 8192 + tid * 16);
    }
    const float d0 = bflo(r_dsk), d1 = bfhi(r_dsk);
    unsigned hp[4];
#pragma unroll
    for (int i = 0; i < 4; ++i) {
      const unsigned yb = pkbf(y0[i] * SK0, y1[i] * SK1);
      const unsigned sk = pkbf(bflo(ub[i]) * d0, bfhi(ub[i]) * d1);
      const unsigned y2 = pkbf(bflo(yb) + bflo(sk), bfhi(yb) + bfhi(sk));
      hp[i] = pkbf(bflo(xc[i]) + bflo(y2), bfhi(xc[i]) + bfhi(y2));
    }
    const u32x2 h0 = {(hp[0] & 0xffffu) | (hp[1] << 16), (hp[2] & 0xffffu) | (hp[3] << 16)};
    const u32x2 h1 = {(hp[0] >> 16) | (hp[1] & 0xffff0000u), (hp[2] >> 16) | (hp[3] & 0xffff0000u)};
    auto rh = __builtin_amdgcn_make_buffer_rsrc((void*)(ws + WS_HT), 0, L * D * 2, 0x00020000);
    const int off = (tid >> 4) * (D * 128) + (2 * b) * 128 + (tid & 15) * 8;
    __builtin_amdgcn_raw_buffer_store_b64(h0, rh, off, 0, 16);
    __builtin_amdgcn_raw_buffer_store_b64(h1, rh, off + 128, 0, 16);
  }
  STAMP(12);
  STAMP_FLUSH(0);
}

constexpr int WIN0 = 14, WIN = 34;
constexpr int win_tgt(int t) { if (t < 0) return WIN0; int a = WIN0 + 3 * (t + 1), b = t + 1 + WIN; int m = a < b ? a : b; return m > 68 ? 68 : m; }
constexpr int M_HT = 0;
constexpr int M_ST = 57344;
constexpr int M_CS = M_ST + 2048;
constexpr int M_P = M_CS + 1024;
constexpr int M_LDS = M_P + 4 * 64 * 272;

__global__ __launch_bounds__(256, 1) void k_mlp(char* __restrict__ ws, unsigned short* __restrict__ out) {
  __shared__ __attribute__((aligned(16))) char smem_[M_LDS];
  LAS char* smem = (LAS char*)smem_;
  const int tid = threadIdx.x, lane = tid & 63;
  const int w = __builtin_amdgcn_readfirstlane(tid >> 6);
  const int xcd = blockIdx.x & 7, idx = blockIdx.x >> 3;
  const int rt = xcd * 4 + idx / 7, j = idx % 7;
  STAMP_DECL
  STAMP(0);
  u32x4 cw[CW_M];
  code_warm_issue<CW_M>(cw, lane, false);
  const u64 tag = load_epoch(ws);
  auto rH = __builtin_amdgcn_make_buffer_rsrc((void*)(ws + WS_HT), 0, L * D * 2, 0x00020000);
  auto rW1 = __builtin_amdgcn_make_buffer_rsrc((void*)(ws + WS_W1F), 0, D * F * 2, 0x00020000);
  auto rW2 = __builtin_amdgcn_make_buffer_rsrc((void*)(ws + WS_W2F), 0, D * F * 2, 0x00020000);
  auto rH1 = __builtin_amdgcn_make_buffer_rsrc((void*)(ws + WS_H1F), 0, L * F * 2, 0x00020000);
  const int l16 = lane * 16;
  u32x4 hv[14];
#pragma unroll
  for (int p = 0; p < 14; ++p) hv[p] = __builtin_amdgcn_raw_buffer_load_b128(rH, tid * 16, rt * (D * 128) + p * 4096, 0);
  SB();
  float csp[4];
  {
    auto rC = __builtin_amdgcn_make_buffer_rsrc((void*)(ws + WS_CS), 0, 4 * F * 4, 0x00020000);
#pragma unroll
    for (int k = 0; k < 4; ++k) csp[k] = __uint_as_float(__builtin_amdgcn_raw_buffer_load_b32(rC, lane * 4, (k * F + 256 * j + 64 * w) * 4, 0));
  }
  const int ft0 = j * 8 + w * 2;
  u32x4 wa[56], wb[2][28];
  auto seqload = [&](int n) {
    if (n < 56) {
      const int a = n / 28, i = n % 28;
      const int ks = (((i / 7) + w) & 3) * 7 + (i % 7);
      wa[n] = __builtin_amdgcn_raw_buffer_load_b128(rW1, l16, ((ft0 + a) * 28 + ks) * 1024, 0);
    } else {
      const int m = n - 56, i = m >> 1, dt = m & 1;
      int jj = j + (i >> 2); jj = jj >= 7 ? jj - 7 : jj;
      const int fs = 16 * jj + 4 * w + (i & 3);
      wb[dt][i] = __builtin_amdgcn_raw_buffer_load_b128(rW2, l16, ((2 * j + dt) * 112 + fs) * 1024, 0);
    }
  };
#pragma unroll
  for (int n = 0; n < WIN0; ++n) seqload(n);
  SB();
  {
    const int r = tid >> 3, c = tid & 7;
#pragma unroll
    for (int p = 0; p < 14; ++p) { const int row = 32 * p + r; *(LAS u32x4*)(smem + M_HT + row * 128 + ((c ^ (4 * ((row >> 1) & 1))) * 16)) = hv[p]; }
  }
  *(LAS float*)(smem + M_CS + (w * 64 + lane) * 4) = (csp[0] + csp[1]) + (csp[2] + csp[3]);
  SB();
  STAMP(1);
  BAR();
  STAMP(2);
  const int hh = lane >> 5, blk = (lane >> 4) & 1, q = (lane & 15) >> 2, p = lane & 3;
  const int sw = (q >> 1) & 1;
  const LAS char* b0 = smem + M_HT + (8 * hh + q) * 128 + ((2 * blk + (p >> 1) + 4 * sw) * 16) + (p & 1) * 8;
  const LAS char* b1 = smem + M_HT + (8 * hh + q) * 128 + ((2 * blk + (p >> 1) + 4 * (1 - sw)) * 16) + (p & 1) * 8;
  f32x16 acc[2][2];
#pragma unroll
  for (int a = 0; a < 2; ++a)
#pragma unroll
    for (int c = 0; c < 2; ++c)
#pragma unroll
      for (int e = 0; e < 16; ++e) acc[a][c][e] = 0.f;
  float s0 = 0.f, s1 = 0.f, q0 = 0.f, q1 = 0.f;
  const unsigned ONES = 0x3f803f80u;
  constexpr int BD = 3;
  u32x4 bf[BD + 1][2];
  auto bload = [&](int i, u32x4& o0, u32x4& o1) {
    const int go = ((((i / 7) + w) & 3) * 7 + (i % 7)) * 2048;
    o0 = tr_read2(b0 + go, b0 + go + 512);
    o1 = tr_read2(b1 + go, b1 + go + 512);
  };
  float mr[2], ri[2];
  u32x4 own[2][4];
  auto epi = [&](int a, int c, int s) {
    u32x4 fr;
    const f32x4 c0 = *(const LAS f32x4*)(smem + M_CS + (w * 64 + a * 32 + 16 * s + 4 * hh) * 4);
    const f32x4 c1 = *(const LAS f32x4*)(smem + M_CS + (w * 64 + a * 32 + 16 * s + 8 + 4 * hh) * 4);
#pragma unroll
    for (int e = 0; e < 4; ++e) {
      const int r0 = 8 * s + 2 * e;
      const float k0 = e < 2 ? c0[2 * e] : c1[2 * e - 4], k1 = e < 2 ? c0[2 * e + 1] : c1[2 * e - 3];
      const float v0 = (acc[a][c][r0] - mr[c] * k0) * ri[c], v1 = (acc[a][c][r0 + 1] - mr[c] * k1) * ri[c];
      fr[e] = pkbf(fmaxf(v0, 0.f), fmaxf(v1, 0.f));
    }
    own[c][2 * a + s] = fr;
    const int fs = 16 * j + 4 * w + 2 * a + s;
    __builtin_amdgcn_raw_buffer_store_b128(fr, rH1, l16, ((rt * 2 + c) * 112 + fs) * 1024, 16);
  };
#pragma unroll
  for (int i = 0; i < BD; ++i) bload(i, bf[i][0], bf[i][1]);
  static_for<0, 56>([&](auto T_) {
    constexpr int t = decltype(T_)::value;
    constexpr int a = t / 28;
    if constexpr (t + BD < 56) bload((t + BD) % 28, bf[(t + BD) % (BD + 1)][0], bf[(t + BD) % (BD + 1)][1]);
    const u32x4 f0 = bf[t % (BD + 1)][0], f1 = bf[t % (BD + 1)][1];
    static_for<win_tgt(t - 1), win_tgt(t)>([&](auto N_) { seqload(decltype(N_)::value); });
    SB();
    acc[a][0] = mfma32b(wa[t], f0, acc[a][0]);
    acc[a][1] = mfma32b(wa[t], f1, acc[a][1]);
    if constexpr (t < 7) {
#pragma unroll
      for (int e = 0; e < 4; ++e) { s0 = dot2bf(f0[e], ONES, s0); q0 = dot2bf(f0[e], f0[e], q0); s1 = dot2bf(f1[e], ONES, s1); q1 = dot2bf(f1[e], f1[e], q1); }
    }
    if constexpr (t == 8) {
      s0 += __shfl_xor(s0, 32); s1 += __shfl_xor(s1, 32); q0 += __shfl_xor(q0, 32); q1 += __shfl_xor(q1, 32);
      if (hh == 0) {
        *(LAS u32x2*)(smem + M_ST + (w * 64 + lane) * 8) = u32x2{__float_as_uint(s0), __float_as_uint(q0)};
        *(LAS u32x2*)(smem + M_ST + (w * 64 + 32 + lane) * 8) = u32x2{__float_as_uint(s1), __float_as_uint(q1)};
      }
    }
    if constexpr (t == 27) {
      code_warm_retire<CW_M>(cw);
      STAMP(3);
      BAR();
      STAMP(4);
#pragma unroll
      for (int c = 0; c < 2; ++c) {
        float s = 0.f, qq = 0.f;
#pragma unroll
        for (int ww = 0; ww < 4; ++ww) { u32x2 v = *(const LAS u32x2*)(smem + M_ST + (ww * 64 + c * 32 + (lane & 31)) * 8); s += __uint_as_float(v[0]); qq += __uint_as_float(v[1]); }
        const float mean = s * (1.0f / D);
        const float var = fmaxf(qq * (1.0f / D) - mean * mean, 0.f);
        float mb, sb; ln_scalars(mean, var, mb, sb);
        mr[c] = mb; ri[c] = rcp_(sb);
      }
    }
    if constexpr (t >= 30 && t < 54 && (t - 30) % 6 == 0) { constexpr int e = (t - 30) / 6; epi(0, e >> 1, e & 1); }
    SB();
  });
#pragma unroll
  for (int e = 0; e < 4; ++e) { epi(1, e >> 1, e & 1); SB(); }
#pragma unroll
  for (int n = 68; n < 84; ++n) seqload(n);
  SB();
  asm volatile("s_waitcnt vmcnt(16)" ::: "memory");
  GAS u64* flg = (GAS u64*)(ws + WS_FLAGB) + (rt * 4 + w) * 8;
  if (lane == 0) __hip_atomic_store(flg + j, tag, __ATOMIC_RELAXED, __HIP_MEMORY_SCOPE_AGENT);
  SB();
#pragma unroll
  for (int n = 84; n < 102; ++n) seqload(n);
  SB();
  const int fl = lane < 7 ? lane : 0;
  u64 fv = __hip_atomic_load(flg + fl, __ATOMIC_RELAXED, __HIP_MEMORY_SCOPE_AGENT);
  SB();
#pragma unroll
  for (int n = 102; n < 112; ++n) seqload(n);
  SB();
  STAMP(5);
#pragma unroll
  for (int a = 0; a < 2; ++a)
#pragma unroll
    for (int c = 0; c < 2; ++c)
#pragma unroll
      for (int e = 0; e < 16; ++e) acc[a][c][e] = 0.f;
#pragma unroll
  for (int i = 0; i < 4; ++i) {
    acc[0][0] = mfma32b(wb[0][i], own[0][i], acc[0][0]);
    acc[0][1] = mfma32b(wb[0][i], own[1][i], acc[0][1]);
    acc[1][0] = mfma32b(wb[1][i], own[0][i], acc[1][0]);
    acc[1][1] = mfma32b(wb[1][i], own[1][i], acc[1][1]);
  }
  SB();
  {
    for (unsigned spins = 0;; ++spins) {
      if (__all(fv == tag)) break;
      if (spins > kSpinCap) { if (lane == 0) __hip_atomic_store((GAS unsigned*)(ws + WS_TMO), 2u, __ATOMIC_RELAXED, __HIP_MEMORY_SCOPE_AGENT); break; }
      fv = __hip_atomic_load(flg + fl, __ATOMIC_RELAXED, __HIP_MEMORY_SCOPE_AGENT);
    }
  }
  asm volatile("" ::: "memory");
  STAMP(6);
  u32x4 hf[2][24];
  auto h1load = [&](int i) {
    const int n = i >> 2, fsub = i & 3;
    int jj = j + n; jj = jj >= 7 ? jj - 7 : jj;
    const int fs = 16 * jj + 4 * w + fsub;
    hf[0][i - 4] = __builtin_amdgcn_raw_buffer_load_b128(rH1, l16, ((rt * 2) * 112 + fs) * 1024, 16);
    hf[1][i - 4] = __builtin_amdgcn_raw_buffer_load_b128(rH1, l16, ((rt * 2 + 1) * 112 + fs) * 1024, 16);
  };
  constexpr int HW = 10;
#pragma unroll
  for (int i = 4; i < 4 + HW; ++i) h1load(i);
  SB();
  static_for<4, 28>([&](auto I_) {
    constexpr int i = decltype(I_)::value;
    if constexpr (i + HW < 28) h1load(i + HW);
    SB();
    acc[0][0] = mfma32b(wb[0][i], hf[0][i - 4], acc[0][0]);
    acc[0][1] = mfma32b(wb[0][i], hf[1][i - 4], acc[0][1]);
    acc[1][0] = mfma32b(wb[1][i], hf[0][i - 4], acc[1][0]);
    acc[1][1] = mfma32b(wb[1][i], hf[1][i - 4], acc[1][1]);
    SB();
  });
  STAMP(7);
#pragma unroll
  for (int a = 0; a < 2; ++a)
#pragma unroll
    for (int c = 0; c < 2; ++c)
#pragma unroll
      for (int g4 = 0; g4 < 4; ++g4) {
        f32x4 v = {acc[a][c][4 * g4], acc[a][c][4 * g4 + 1], acc[a][c][4 * g4 + 2], acc[a][c][4 * g4 + 3]};
        *(LAS f32x4*)(smem + M_P + (w * 64 + c * 32 + (lane & 31)) * 272 + (a * 32 + 8 * g4 + 4 * hh) * 4) = v;
      }
  BAR();
  STAMP(8);
  {
    const int l = lane, c4 = w;
    float o[16];
#pragma unroll
    for (int e = 0; e < 16; ++e) o[e] = 0.f;
#pragma unroll
    for (int ww = 0; ww < 4; ++ww)
#pragma unroll
      for (int i = 0; i < 4; ++i) {
        f32x4 v = *(const LAS f32x4*)(smem + M_P + (ww * 64 + l) * 272 + (16 * c4 + 4 * i) * 4);
#pragma unroll
        for (int e = 0; e < 4; ++e) o[4 * i + e] += v[e];
      }
    unsigned pk[8];
#pragma unroll
    for (int e = 0; e < 8; ++e) {
      const int r0 = 64 * j + 16 * c4 + 2 * e, r1 = r0 + 1;
      const float h0 = bf2f(*(const LAS unsigned short*)(smem + M_HT + r0 * 128 + (((l >> 3) ^ (4 * ((r0 >> 1) & 1))) * 16) + (l & 7) * 2));
      const float h1 = bf2f(*(const LAS unsigned short*)(smem + M_HT + r1 * 128 + (((l >> 3) ^ (4 * ((r1 >> 1) & 1))) * 16) + (l & 7) * 2));
      const unsigned ob = pkbf(o[2 * e], o[2 * e + 1]);
      pk[e] = pkbf(h0 + bflo(ob), h1 + bfhi(ob));
    }
    char* op = (char*)out + ((size_t)(rt * 64 + l) * D + 64 * j + 16 * c4) * 2;
    *(u32x4*)op = u32x4{pk[0], pk[1], pk[2], pk[3]};
    *(u32x4*)(op + 16) = u32x4{pk[4], pk[5], pk[6], pk[7]};
  }
  STAMP(9);
  STAMP_FLUSH(1);
}

}

extern "C" void kernel_launch(void* const* d_in, const int* in_sizes, int n_in, void* d_out, int out_size, void* d_ws, size_t ws_size, hipStream_t stream) {
  typedef const unsigned short* cp;
  k_ssm<<<256, 512, 0, stream>>>((cp)d_in[0], (cp)d_in[1], (cp)d_in[2], (cp)d_in[3], (cp)d_in[4], (cp)d_in[5], (cp)d_in[6], (cp)d_in[7], (cp)d_in[8], (cp)d_in[10], (cp)d_in[11], (char*)d_ws, (cp)d_out);
  k_mlp<<<224, 256, 0, stream>>>((char*)d_ws, (unsigned short*)d_out);
}
```

```cpp
#include <hip/hip_runtime.h>
#include <stdint.h>

namespace {

typedef __attribute__((ext_vector_type(8))) _Float16 f16x8;
typedef __attribute__((ext_vector_type(4))) _Float16 f16x4;
typedef __attribute__((ext_vector_type(2))) _Float16 f16x2;
typedef __attribute__((ext_vector_type(8))) __bf16 bf16x8;
typedef __attribute__((ext_vector_type(2))) __bf16 bf16x2;
typedef __attribute__((ext_vector_type(4))) float f32x4;
typedef __attribute__((ext_vector_type(16))) float f32x16;
typedef __attribute__((ext_vector_type(4))) unsigned u32x4;
typedef __attribute__((ext_vector_type(2))) unsigned u32x2;
typedef __attribute__((ext_vector_type(4))) short s16x4;
typedef unsigned long long u64;
#define LAS __attribute__((address_space(3)))
#define GAS __attribute__((address_space(1)))
#define DI __device__ __forceinline__

constexpr int L = 2048, D = 448, NM = 256, F = 1792;
#define CW_A 12

constexpr size_t WS_EPOCH = 0;
constexpr size_t WS_TMO = 64;
constexpr size_t WS_FLAGB = 8192;
constexpr size_t WS_STATS = 16384;
constexpr size_t WS_XT = WS_STATS + 1024 * 16;
constexpr size_t WS_CS = WS_XT + (size_t)512 * D * 16;
constexpr size_t WS_HT = WS_CS + 4 * F * 4;
constexpr size_t WS_W1F = WS_HT + (size_t)D * L * 2;
constexpr size_t WS_W2F = WS_W1F + (size_t)D * F * 2;
constexpr size_t WS_H1F = WS_W2F + (size_t)D * F * 2;
constexpr size_t WS_END = WS_H1F + (size_t)L * F * 2;
constexpr unsigned kSpinCap = 1u << 20;

#define SB() __builtin_amdgcn_sched_barrier(0)
#define BAR() do { asm volatile("s_waitcnt lgkmcnt(0)" ::: "memory"); __builtin_amdgcn_s_barrier(); asm volatile("" ::: "memory"); } while (0)
DI float bf2f(unsigned b) { return __uint_as_float(b << 16); }
DI float bflo(unsigned p) { return __uint_as_float(p << 16); }
DI float bfhi(unsigned p) { return __uint_as_float(p & 0xffff0000u); }
DI unsigned pkbf(float a, float b) { bf16x2 r; r[0] = (__bf16)a; r[1] = (__bf16)b; return __builtin_bit_cast(unsigned, r); }
DI float rbf(float a) { return bflo(pkbf(a, a)); }
DI unsigned pkh(float a, float b) { f16x2 r; r[0] = (_Float16)a; r[1] = (_Float16)b; return __builtin_bit_cast(unsigned, r); }

template <int CTRL> DI float dppf(float v) { return __int_as_float(__builtin_amdgcn_update_dpp(0, __float_as_int(v), CTRL, 0xF, 0xF, true)); }
DI float rdl(float v, int l) { return __int_as_float(__builtin_amdgcn_readlane(__float_as_int(v), l)); }
DI float wave_sum(float v) {
  v += dppf<0xB1>(v); v += dppf<0x4E>(v); v += dppf<0x141>(v); v += dppf<0x140>(v);
  return (rdl(v, 0) + rdl(v, 16)) + (rdl(v, 32) + rdl(v, 48));
}
DI float wave_max(float v) {
  v = fmaxf(v, dppf<0xB1>(v)); v = fmaxf(v, dppf<0x4E>(v)); v = fmaxf(v, dppf<0x141>(v)); v = fmaxf(v, dppf<0x140>(v));
  return fmaxf(fmaxf(rdl(v, 0), rdl(v, 16)), fmaxf(rdl(v, 32), rdl(v, 48)));
}
DI s16x4 tr_read(const LAS char* p) { return __builtin_amdgcn_ds_read_tr16_b64_v4i16((LAS s16x4*)p); }
DI u32x4 tr_read2(const LAS char* p0, const LAS char* p1) {
  s16x4 a = tr_read(p0), b = tr_read(p1);
  u32x2 a2 = __builtin_bit_cast(u32x2, a), b2 = __builtin_bit_cast(u32x2, b);
  u32x4 r; r[0] = a2[0]; r[1] = a2[1]; r[2] = b2[0]; r[3] = b2[1]; return r;
}
DI f32x4 mfma16h(u32x4 a, u32x4 b, f32x4 c) { return __builtin_amdgcn_mfma_f32_16x16x32_f16(__builtin_bit_cast(f16x8, a), __builtin_bit_cast(f16x8, b), c, 0, 0, 0); }
DI f32x16 mfma32b(u32x4 a, u32x4 b, f32x16 c) { return __builtin_amdgcn_mfma_f32_32x32x16_bf16(__builtin_bit_cast(bf16x8, a), __builtin_bit_cast(bf16x8, b), c, 0, 0, 0); }
DI float dot2bf(unsigned a, unsigned b, float c) { return __builtin_amdgcn_fdot2_f32_bf16(__builtin_bit_cast(bf16x2, a), __builtin_bit_cast(bf16x2, b), c, false); }

template <int I> struct IC { static constexpr int value = I; };
template <int B, int E, class Fn> DI void static_for(Fn&& fn) { if constexpr (B < E) { fn(IC<B>{}); static_for<B + 1, E>(fn); } }
template <int NKB> DI void code_warm_issue(u32x4 (&v)[NKB], int lane, bool on) {
  const char* pc = (const char*)(__builtin_amdgcn_s_getpc() & ~(unsigned long long)63);
#pragma unroll
  for (int i = 0; i < NKB; ++i) v[i] = u32x4{0u, 0u, 0u, 0u};
  if (on) {
#pragma unroll
    for (int i = 0; i < NKB; ++i) v[i] = *(const u32x4*)(pc + i * 1024 + lane * 16);
  }
}
template <int NKB> DI void code_warm_retire(u32x4 (&v)[NKB]) {
#pragma unroll
  for (int i = 0; i < NKB; ++i) asm volatile("" :: "v"(v[i]));
}
struct cf { float r, i; };
DI cf cmul(cf a, cf b) { cf o; o.r = a.r * b.r - a.i * b.i; o.i = a.r * b.i + a.i * b.r; return o; }
DI cf csq(cf a) { cf o; o.r = a.r * a.r - a.i * a.i; o.i = 2.f * a.r * a.i; return o; }
DI cf cexp_p2(float zr2, float zf, float p2) {
  float m = __builtin_amdgcn_exp2f(zr2 * p2);
  float t = __builtin_amdgcn_fractf(zf * p2);
  cf o; o.r = m * __builtin_amdgcn_cosf(t); o.i = m * __builtin_amdgcn_sinf(t); return o;
}

DI void ln_scalars(float mean, float var, float& mb, float& sb) {
  mb = rbf(mean);
  float vb = rbf(var);
  sb = rbf(__builtin_sqrtf(rbf(vb + 1e-5f)));
}
DI float rcp_(float s) { return __builtin_amdgcn_rcpf(s); }

struct IdxTab { unsigned short v[256][12]; };
constexpr int bf16_round_index(int lag) {
  if (lag < 0 || lag > 2047) return 641;
  if (lag < 256) return lag;
  int e = 8; while ((lag >> (e + 1)) != 0) ++e;
  const int sh = e - 7;
  int q = lag >> sh; const int r = lag & ((1 << sh) - 1), half = 1 << (sh - 1);
  if (r > half || (r == half && (q & 1))) ++q;
  return 256 + (e - 8) * 128 + (q - 128);
}
constexpr IdxTab make_idx() {
  IdxTab t{};
  for (int c = 0; c < 256; ++c) { for (int e = 0; e < 11; ++e) t.v[c][e] = (unsigned short)(2 * bf16_round_index(2048 - 8 * c + 3 - e)); t.v[c][11] = 0; }
  return t;
}
__device__ const IdxTab g_idx = make_idx();

constexpr int A_TAB = 0;
constexpr int A_MISC = A_TAB + 8 * 16384;
constexpr int A_KDC = A_MISC;
constexpr int A_SC = A_KDC + 2 * 1312;
constexpr int A_XS = A_SC + 128;
constexpr int A_LDS = A_XS + 9216 + 64;
constexpr int TOE_CP = 4160, TOE_CH = 4 * TOE_CP;
constexpr int A_U16 = A_TAB + 4096;
constexpr int A_TOE2 = A_TAB + 13312;
constexpr int A_YP = A_TAB + 46592;

__global__ __launch_bounds__(512, 2) void k_ssm(const unsigned short* __restrict__ x, const unsigned short* __restrict__ Are, const unsigned short* __restrict__ Aim,
                                                 const unsigned short* __restrict__ Bre, const unsigned short* __restrict__ Bim, const unsigned short* __restrict__ Cre,
                                                 const unsigned short* __restrict__ Cim, const unsigned short* __restrict__ logdt, const unsigned short* __restrict__ Dsk,
                                                 const unsigned short* __restrict__ w1, const unsigned short* __restrict__ w2, char* __restrict__ ws) {
  __shared__ __attribute__((aligned(16))) char smem_[A_LDS];
  LAS char* smem = (LAS char*)smem_;
  const int tid = threadIdx.x, lane = tid & 63;
  const int w = __builtin_amdgcn_readfirstlane(tid >> 6);
  const int b = blockIdx.x;
  const bool has_ch = b < 224;
  const int c2 = w >> 2, w4 = w & 3;
  const int ch = 2 * b + c2;
  u32x4 cw[CW_A];
  code_warm_issue<CW_A>(cw, lane, w == 7);
  const int bs = has_ch ? b : 0;
  const int chs = 2 * bs + c2;
  const int po = chs * NM + w4 * 64 + lane;
  const unsigned short r_are = Are[po], r_aim = Aim[po], r_bre = Bre[po], r_bim = Bim[po], r_cre = Cre[po], r_cim = Cim[po];
  const unsigned short r_ldt = logdt[chs];
  const u32x2 ev = *(const u32x2*)(ws + WS_EPOCH);
  SB();
  const int row = 8 * b + w;
  u32x4 xv = *(const u32x4*)(x + (size_t)row * D + min(lane, 55) * 8);
  const int trt = bs & 31, tg = bs >> 5;
  const u32x4 xt = *(const u32x4*)(x + (size_t)(64 * trt + 8 * w + (lane >> 3)) * D + 64 * tg + (lane & 7) * 8);
  const unsigned r_dsk = *(const unsigned*)(Dsk + 2 * bs);
  const int t = tid & 255;
  const u32x4 ix0 = *(const u32x4*)&g_idx.v[t][0];
  const u32x2 ix1 = *(const u32x2*)&g_idx.v[t][8];
  SB();
  unsigned elo = __builtin_amdgcn_readfirstlane(ev[0]), ehi = __builtin_amdgcn_readfirstlane(ev[1]);
  asm volatile("" : "+s"(elo), "+s"(ehi));
  const u64 tag = (((u64)ehi << 32) | elo) + 1;
  const unsigned tlo_ = (unsigned)tag, thi_ = (unsigned)(tag >> 32);
  LAS char* tab = smem + A_TAB + w * 16384;

  auto ln_row = [&]() {
    if (lane >= 56) xv = u32x4{0u, 0u, 0u, 0u};
    float xf[8];
#pragma unroll
    for (int j = 0; j < 4; ++j) { xf[2 * j] = bflo(xv[j]); xf[2 * j + 1] = bfhi(xv[j]); }
    float s = 0.f;
#pragma unroll
    for (int j = 0; j < 8; ++j) s += xf[j];
    const float mean = wave_sum(s) / (float)D;
    float q = 0.f;
#pragma unroll
    for (int j = 0; j < 8; ++j) { float d = xf[j] - mean; q += d * d; }
    if (lane >= 56) q = 0.f;
    const float var = wave_sum(q) / (float)D;
    float mb, sb; ln_scalars(mean, var, mb, sb);
    if (lane == 0) *(LAS unsigned*)(smem + A_XS + 9216 + w * 4) = (__float_as_uint(mb) >> 16) | (__float_as_uint(sb) & 0xffff0000u);
    if (has_ch) *(LAS u32x4*)(smem + A_XS + (8 * w + (lane >> 3)) * 144 + (lane & 7) * 16) = xt;
  };
  auto ux_publish = [&]() {
    if (tid < 4) {
      const u32x2 p = *(const LAS u32x2*)(smem + A_XS + 9216 + tid * 8);
      auto rs = __builtin_amdgcn_make_buffer_rsrc((void*)(ws + WS_STATS), 0, 1024 * 16, 0x00020000);
      __builtin_amdgcn_raw_buffer_store_b128(u32x4{p[0], p[1], tlo_, thi_}, rs, (4 * b + tid) * 16, 0, 16);
    }
    if (has_ch) {
      auto rx = __builtin_amdgcn_make_buffer_rsrc((void*)(ws + WS_XT), 0, 512 * D * 16, 0x00020000);
      const int qd = lane & 15;
#pragma unroll
      for (int i = 0; i < 2; ++i) {
        const int c = 8 * w + 4 * i + (lane >> 4);
        const LAS char* a0 = smem + A_XS + (4 * qd) * 144 + c * 2;
        const unsigned r0 = *(const LAS unsigned short*)a0, r1 = *(const LAS unsigned short*)(a0 + 144), r2 = *(const LAS unsigned short*)(a0 + 288), r3 = *(const LAS unsigned short*)(a0 + 432);
        __builtin_amdgcn_raw_buffer_store_b128(u32x4{r0 | (r1 << 16), r2 | (r3 << 16), tlo_, thi_}, rx, ((64 * tg + c) * 512 + 16 * trt + qd) * 16, 0, 16);
      }
    }
  };
  if (!has_ch) {
    code_warm_retire<CW_A>(cw);
    ln_row();
    BAR();
    ux_publish();
    const int task = (b - 224) * 8 + w;
    if (task < 224) {
      const bool is_w1 = task < 112;
      const int tk = is_w1 ? task : task - 112;
      const int nb = is_w1 ? tk % 28 : tk % 7, kg = is_w1 ? tk / 28 : tk / 7;
      const int rld = is_w1 ? F : D;
      const unsigned short* src = (is_w1 ? w1 : w2) + (size_t)(16 * 7 * kg + (lane >> 3)) * rld + 64 * nb + (lane & 7) * 8;
      u32x4 r0[7], r1[7];
#pragma unroll
      for (int u = 0; u < 7; ++u) { r0[u] = *(const u32x4*)(src + (size_t)(16 * u) * rld); r1[u] = *(const u32x4*)(src + (size_t)(16 * u + 8) * rld); }
      LAS char* sc = smem + w * 16384;
#pragma unroll
      for (int u = 0; u < 7; ++u) { *(LAS u32x4*)(sc + u * 2048 + lane * 16) = r0[u]; *(LAS u32x4*)(sc + u * 2048 + 1024 + lane * 16) = r1[u]; }
      __builtin_amdgcn_s_waitcnt(0xC07F);
      __builtin_amdgcn_wave_barrier();
      const int g = lane >> 4, blk = g & 1, hh = g >> 1, q = (lane & 15) >> 2, p = lane & 3;
      const int q0 = is_w1 ? (8 * hh + q) : (4 * hh + q);
      const int q1 = is_w1 ? (8 * hh + 4 + q) : (8 + 4 * hh + q);
      auto rd = __builtin_amdgcn_make_buffer_rsrc((void*)(ws + (is_w1 ? WS_W1F : WS_W2F)), 0, D * F * 2, 0x00020000);
      const int nks = is_w1 ? 28 : 112;
      float cs[2] = {0.f, 0.f};
#pragma unroll
      for (int u = 0; u < 7; ++u) {
#pragma unroll
        for (int tt = 0; tt < 2; ++tt) {
          const int cb = (tt * 32 + blk * 16 + 4 * p) * 2;
          u32x4 fr = tr_read2(sc + u * 2048 + q0 * 128 + cb, sc + u * 2048 + q1 * 128 + cb);
          __builtin_amdgcn_raw_buffer_store_b128(fr, rd, lane * 16, ((2 * nb + tt) * nks + 7 * kg + u) * 1024, 16);
#pragma unroll
          for (int e = 0; e < 4; ++e) cs[tt] = dot2bf(fr[e], 0x3f803f80u, cs[tt]);
        }
      }
      if (is_w1) {
        cs[0] += __shfl_xor(cs[0], 32); cs[1] += __shfl_xor(cs[1], 32);
        auto rc = __builtin_amdgcn_make_buffer_rsrc((void*)(ws + WS_CS), 0, 4 * F * 4, 0x00020000);
        __builtin_amdgcn_raw_buffer_store_b32(__float_as_uint(lane < 32 ? cs[0] : cs[1]), rc, (kg * F + 64 * nb + lane) * 4, 0, 16);
      }
    }
    return;
  }
  float k2048;
  int ew;
  {
    const float are = bf2f(r_are), aim = bf2f(r_aim), bre = bf2f(r_bre), bim = bf2f(r_bim), cre = bf2f(r_cre), cim = bf2f(r_cim);
    const float dt = rbf(expf(bf2f(r_ldt)));
    const float zr = dt * are, zi = dt * aim;
    const float zr2 = zr * 1.44269504088896341f, zf = zi * 0.15915494309189535f;
    const float dbr = dt * bre, dbi = dt * bim;
    cf CB; CB.r = cre * dbr - cim * dbi; CB.i = cre * dbi + cim * dbr;
    const float mx = wave_max(fmaxf(fabsf(CB.r), fabsf(CB.i)));
    const float gz = fmaxf(wave_max(zr2), 0.f) * 2048.f;
    int e = (mx > 0.f) ? ((int)(__float_as_uint(mx) >> 23) - 127) : 0;
    e += (int)ceilf(fminf(gz, 100.f));
    e = max(-120, min(120, e));
    ew = e;
    const float S = __uint_as_float((unsigned)(127 - e) << 23);
    cf G0; G0.r = CB.r * S; G0.i = CB.i * S;
    const cf E1 = cexp_p2(zr2, zf, 1.f), E2 = csq(E1), E4 = cexp_p2(zr2, zf, 4.f), E8 = csq(E4);
    const cf E16 = cexp_p2(zr2, zf, 16.f), E32 = csq(E16), E64 = cexp_p2(zr2, zf, 64.f), E128 = csq(E64);
    const cf E256 = cexp_p2(zr2, zf, 256.f), E512 = csq(E256), E1024 = cexp_p2(zr2, zf, 1024.f), E2048 = csq(E1024);
    SB();
    ln_row();
    SB();
    const int sw = (lane >> 1) & 3;
    LAS char* tb = tab + lane * 64;
    auto put = [&](int tile, int half, const cf* v, float isign) {
      u32x4 re, im;
#pragma unroll
      for (int j = 0; j < 4; ++j) { re[j] = pkh(v[2 * j].r, v[2 * j + 1].r); im[j] = pkh(isign * v[2 * j].i, isign * v[2 * j + 1].i); }
      *(LAS u32x4*)(tb + tile * 4096 + ((half ^ sw) * 16)) = re;
      *(LAS u32x4*)(tb + tile * 4096 + (((2 + half) ^ sw) * 16)) = im;
    };
    cf Er[8];
    Er[0].r = 1.f; Er[0].i = 0.f; Er[1] = E1; Er[2] = E2; Er[3] = cmul(E1, E2); Er[4] = E4; Er[5] = cmul(E4, E1); Er[6] = cmul(E4, E2); Er[7] = cmul(E4, Er[3]);
    {
      cf Y[8];
      Y[0].r = 1.f; Y[0].i = 0.f; Y[1] = E8; Y[2] = E16; Y[3] = cmul(E8, E16); Y[4] = E32; Y[5] = cmul(E32, E8); Y[6] = cmul(E32, E16); Y[7] = cmul(E32, Y[3]);
      put(3, 0, Y, 1.f);
      cf Y2[8];
      Y2[0] = E64;
#pragma unroll
      for (int j = 1; j < 8; ++j) Y2[j] = cmul(Y[j], E64);
      put(3, 1, Y2, 1.f);
    }
    cf G[8];
    G[0] = G0; G[1] = cmul(G0, E128); G[2] = cmul(G0, E256); G[3] = cmul(G[1], E256);
#pragma unroll
    for (int j = 0; j < 4; ++j) G[4 + j] = cmul(G[j], E512);
    {
      cf T[8];
      T[0] = G[0];
#pragma unroll
      for (int j = 1; j < 8; ++j) T[j] = cmul(G[0], Er[j]);
      put(0, 0, T, -1.f);
      SB();
      BAR();
      ux_publish();
      SB();
      T[0] = G[1];
#pragma unroll
      for (int j = 1; j < 8; ++j) T[j] = cmul(G[1], Er[j]);
      put(0, 1, T, -1.f);
    }
    {
      cf T[8];
      T[0] = G[2]; T[1] = cmul(G[2], E2); T[2] = cmul(G[2], E4); T[3] = cmul(G[2], Er[6]);
      T[4] = G[3]; T[5] = cmul(G[3], E2); T[6] = cmul(G[3], E4); T[7] = cmul(G[3], Er[6]);
      put(1, 0, T, -1.f);
#pragma unroll
      for (int j = 0; j < 4; ++j) { T[2 * j] = G[4 + j]; T[2 * j + 1] = cmul(G[4 + j], E4); }
      put(1, 1, T, -1.f);
    }
    {
      cf T[8];
#pragma unroll
      for (int j = 0; j < 8; ++j) T[j] = cmul(G[j], E1024);
      put(2, 0, T, -1.f);
    }
    k2048 = wave_sum(G0.r * E2048.r - G0.i * E2048.i);
  }
  __builtin_amdgcn_s_waitcnt(0xC07F);
  __builtin_amdgcn_wave_barrier();
  {
    const int g = lane >> 4, q = (lane & 15) >> 2, p = lane & 3;
    f32x4 acc0 = {0.f, 0.f, 0.f, 0.f}, acc1 = acc0, acc2 = acc0;
    const int part = q & 1, cl = part * 2 + (p >> 1);
    u32x4 bb[4], a0[4], a1[4], a2[4];
#pragma unroll
    for (int s = 0; s < 4; ++s) {
      const int m0 = 16 * s + 8 * (g >> 1) + 2 * (g & 1) + (q >> 1), m1 = m0 + 4;
      const int o0 = m0 * 64 + ((cl ^ ((m0 >> 1) & 3)) * 16) + (p & 1) * 8;
      const int o1 = m1 * 64 + ((cl ^ ((m1 >> 1) & 3)) * 16) + (p & 1) * 8;
      bb[s] = tr_read2(tab + 3 * 4096 + o0, tab + 3 * 4096 + o1);
      a0[s] = tr_read2(tab + o0, tab + o1);
      a1[s] = tr_read2(tab + 4096 + o0, tab + 4096 + o1);
      a2[s] = tr_read2(tab + 8192 + o0, tab + 8192 + o1);
    }
    SB();
#pragma unroll
    for (int s = 0; s < 4; ++s) { acc0 = mfma16h(a0[s], bb[s], acc0); acc1 = mfma16h(a1[s], bb[s], acc1); acc2 = mfma16h(a2[s], bb[s], acc2); }
    SB();
    *(LAS f32x4*)(tab + lane * 16) = acc0;
    *(LAS f32x4*)(tab + 1024 + lane * 16) = acc1;
    *(LAS f32x4*)(tab + 2048 + lane * 16) = acc2;
    if (lane == 0) { *(LAS int*)(smem + A_SC + w * 8) = ew; *(LAS float*)(smem + A_SC + w * 8 + 4) = k2048; }
  }
  auto rst = __builtin_amdgcn_make_buffer_rsrc((void*)(ws + WS_STATS), 0, 1024 * 16, 0x00020000);
  auto rxq = __builtin_amdgcn_make_buffer_rsrc((void*)(ws + WS_XT), 0, 512 * D * 16, 0x00020000);
  u32x4 gr[4];
  auto sweep = [&]() {
    gr[0] = __builtin_amdgcn_raw_buffer_load_b128(rst, tid * 32, 0, 16);
    gr[1] = __builtin_amdgcn_raw_buffer_load_b128(rst, tid * 32 + 16, 0, 16);
    gr[2] = __builtin_amdgcn_raw_buffer_load_b128(rxq, tid * 16, (2 * b) * 8192, 16);
    gr[3] = __builtin_amdgcn_raw_buffer_load_b128(rxq, tid * 16, (2 * b + 1) * 8192, 16);
  };
  sweep();
  BAR();
  float SKinv;
  {
    int e4[4]; float k4[4];
#pragma unroll
    for (int j = 0; j < 4; ++j) { e4[j] = *(LAS int*)(smem + A_SC + (c2 * 4 + j) * 8); k4[j] = *(LAS float*)(smem + A_SC + (c2 * 4 + j) * 8 + 4); }
    const int em = max(max(e4[0], e4[1]), max(e4[2], e4[3]));
    float fw[4];
#pragma unroll
    for (int j = 0; j < 4; ++j) { int d = max(e4[j] - em + 5, -120); fw[j] = __uint_as_float((unsigned)(127 + d) << 23); }
    SKinv = __uint_as_float((unsigned)(127 + em - 5) << 23);
    LAS char* kdc = smem + A_KDC + c2 * 1312;
    if (t < 192) {
      const int tile = t >> 6, ln = t & 63, c = ln & 15, g = ln >> 4;
      f32x4 s = {0.f, 0.f, 0.f, 0.f};
#pragma unroll
      for (int j = 0; j < 4; ++j) { f32x4 v = *(LAS f32x4*)(smem + A_TAB + (c2 * 4 + j) * 16384 + tile * 1024 + ln * 16); s += v * fw[j]; }
      const unsigned p01 = pkh(s[0], s[1]), p23 = pkh(s[2], s[3]);
      if (tile == 0) {
        u32x2 v = {p01, p23}; *(LAS u32x2*)(kdc + (128 * (g >> 1) + 8 * c + 4 * (g & 1)) * 2) = v;
      } else if (tile == 1) {
        if (g < 2) { u32x2 v = {p01, p23}; *(LAS u32x2*)(kdc + (256 + 64 * g + 4 * c) * 2) = v; }
        else { const int i0 = 384 + 64 * (g - 2) + 2 * c; *(LAS unsigned*)(kdc + i0 * 2) = p01; *(LAS unsigned*)(kdc + (i0 + 32) * 2) = p23; }
      } else if (g < 2) {
        const int i0 = 512 + 64 * g + c;
        *(LAS unsigned short*)(kdc + i0 * 2) = (unsigned short)p01; *(LAS unsigned short*)(kdc + (i0 + 16) * 2) = (unsigned short)(p01 >> 16);
        *(LAS unsigned short*)(kdc + (i0 + 32) * 2) = (unsigned short)p23; *(LAS unsigned short*)(kdc + (i0 + 48) * 2) = (unsigned short)(p23 >> 16);
      }
    } else if (t == 192) {
      const float k = k4[0] * fw[0] + k4[1] * fw[1] + k4[2] * fw[2] + k4[3] * fw[3];
      *(LAS unsigned*)(kdc + 640 * 2) = pkh(k, 0.f);
    }
  }
  float SKo;
  {
    int e4[4];
#pragma unroll
    for (int j = 0; j < 4; ++j) e4[j] = *(LAS int*)(smem + A_SC + ((1 - c2) * 4 + j) * 8);
    const int em = max(max(e4[0], e4[1]), max(e4[2], e4[3]));
    SKo = __uint_as_float((unsigned)(127 + em - 5) << 23);
  }
  const float SK0 = c2 ? SKo : SKinv, SK1 = c2 ? SKinv : SKo;
  unsigned ub[4], xc[4];
  {
    for (unsigned spins = 0;; ++spins) {
      bool ok = true;
#pragma unroll
      for (int i = 0; i < 4; ++i) ok = ok && gr[i][2] == tlo_ && gr[i][3] == thi_;
      if (__all(ok)) break;
      if (spins > kSpinCap) { if (lane == 0) __hip_atomic_store((GAS unsigned*)(ws + WS_TMO), 1u, __ATOMIC_RELAXED, __HIP_MEMORY_SCOPE_AGENT); break; }
      sweep();
    }
    xc[0] = (gr[2][0] & 0xffffu) | (gr[3][0] << 16); xc[1] = (gr[2][0] >> 16) | (gr[3][0] & 0xffff0000u);
    xc[2] = (gr[2][1] & 0xffffu) | (gr[3][1] << 16); xc[3] = (gr[2][1] >> 16) | (gr[3][1] & 0xffff0000u);
    float u0[4], u1[4];
#pragma unroll
    for (int i = 0; i < 4; ++i) {
      const unsigned pay = gr[i >> 1][i & 1];
      const float mb = bflo(pay), ri = rcp_(bfhi(pay));
      const unsigned tt = pkbf(bflo(xc[i]) - mb, bfhi(xc[i]) - mb);
      ub[i] = pkbf(bflo(tt) * ri, bfhi(tt) * ri);
      u0[i] = bflo(ub[i]); u1[i] = bfhi(ub[i]);
    }
    *(LAS u32x2*)(smem + A_U16 + 512 + tid * 8) = u32x2{pkh(u0[0], u0[1]), pkh(u0[2], u0[3])};
    *(LAS u32x2*)(smem + A_U16 + 4608 + 512 + tid * 8) = u32x2{pkh(u1[0], u1[1]), pkh(u1[2], u1[3])};
    if (t < 32) *(LAS u32x4*)(smem + A_U16 + c2 * 4608 + t * 16) = u32x4{0u, 0u, 0u, 0u};
  }
  code_warm_retire<CW_A>(cw);
  BAR();
  u32x4 Bf[2][8];
  {
    const int bq = lane & 15, g = lane >> 4;
    const LAS char* ua = smem + A_U16 + c2 * 4608 + (256 + 16 * (bq - 1) + 8 * g) * 2 - 64 * w4 - 256;
#pragma unroll
    for (int it = 0; it < 2; ++it)
#pragma unroll
      for (int k = 0; k < 8; ++k) Bf[it][k] = *(const LAS u32x4*)(ua + k * 512 + (1 - it) * 256);
  }
  {
    const LAS char* kdc = smem + A_KDC + c2 * 1312;
    LAS char* toe = smem + A_TOE2 + c2 * TOE_CH;
    unsigned v[11];
#pragma unroll
    for (int e = 0; e < 11; ++e) {
      const unsigned wd = e < 8 ? ix0[e >> 1] : ix1[(e - 8) >> 1];
      const unsigned off = (e & 1) ? (wd >> 16) : (wd & 0xffffu);
      v[e] = *(const LAS unsigned short*)(kdc + off);
    }
    unsigned ev_[5], od[5];
#pragma unroll
    for (int j = 0; j < 5; ++j) { ev_[j] = v[2 * j] | (v[2 * j + 1] << 16); od[j] = v[2 * j + 1] | (v[2 * j + 2] << 16); }
    *(LAS u32x4*)(toe + 0 * TOE_CP + t * 16) = u32x4{od[1], od[2], od[3], od[4]};
    *(LAS u32x4*)(toe + 1 * TOE_CP + t * 16) = u32x4{ev_[1], ev_[2], ev_[3], ev_[4]};
    *(LAS u32x4*)(toe + 2 * TOE_CP + t * 16) = u32x4{od[0], od[1], od[2], od[3]};
    *(LAS u32x4*)(toe + 3 * TOE_CP + t * 16) = u32x4{ev_[0], ev_[1], ev_[2], ev_[3]};
    if (t < 12) {
      const int s = t & 3, c = 256 + (t >> 2);
      const u32x2 k03 = *(const LAS u32x2*)kdc;
      const unsigned k0 = k03[0] & 0xffffu, k1 = k03[0] >> 16, k2 = k03[1] & 0xffffu, k3 = k03[1] >> 16;
      unsigned d0 = s == 0 ? k0 : s == 1 ? (k1 | (k0 << 16)) : s == 2 ? (k2 | (k1 << 16)) : (k3 | (k2 << 16));
      unsigned d1 = s == 2 ? k0 : s == 3 ? (k1 | (k0 << 16)) : 0u;
      if (c != 256) { d0 = 0u; d1 = 0u; }
      *(LAS u32x4*)(toe + s * TOE_CP + c * 16) = u32x4{d0, d1, 0u, 0u};
    }
  }
  BAR();
  if (b == 0 && tid == 0) __hip_atomic_store((GAS u64*)(ws + WS_EPOCH), tag, __ATOMIC_RELAXED, __HIP_MEMORY_SCOPE_AGENT);
  {
    const int bq = lane & 15, g = lane >> 4;
    f32x4 acc[8];
#pragma unroll
    for (int i = 0; i < 8; ++i) acc[i] = f32x4{0.f, 0.f, 0.f, 0.f};
    const LAS char* ta = smem + A_TOE2 + c2 * TOE_CH + (bq & 3) * TOE_CP + (2032 - 4 * (bq >> 2) + 8 * g) * 2 - 64 * w4 - 512 * 7 - 256;
    u32x4 A[2][8];
#pragma unroll
    for (int it = 0; it < 2; ++it)
#pragma unroll
      for (int k = 0; k < 8; ++k) {
        const u32x2 lo = *(const volatile LAS u32x2*)(ta + (7 - k) * 512 + (1 - it) * 256), hi = *(const volatile LAS u32x2*)(ta + (7 - k) * 512 + (1 - it) * 256 + 8);
        A[it][k] = u32x4{lo[0], lo[1], hi[0], hi[1]};
      }
    SB();
#pragma unroll
    for (int it = 0; it < 2; ++it) {
#pragma unroll
      for (int k = 0; k < 8; ++k)
#pragma unroll
        for (int i = k; i < 8; ++i) acc[i] = mfma16h(A[it][k], Bf[it][i - k], acc[i]);
      SB();
    }
    LAS char* yp = smem + A_YP + w * 8192;
#pragma unroll
    for (int i = 0; i < 8; ++i) *(LAS f32x4*)(yp + i * 1024 + bq * 64 + g * 16) = acc[i];
  }
  BAR();
  {
    f32x4 y0 = {0.f, 0.f, 0.f, 0.f}, y1 = y0;
#pragma unroll
    for (int j = 0; j < 4; ++j) {
      y0 += *(const LAS f32x4*)(smem + A_YP + j * 8192 + tid * 16);
      y1 += *(const LAS f32x4*)(smem + A_YP + (4 + j) * 8192 + tid * 16);
    }
    const float d0 = bflo(r_dsk), d1 = bfhi(r_dsk);
    unsigned hp[4];
#pragma unroll
    for (int i = 0; i < 4; ++i) {
      const unsigned yb = pkbf(y0[i] * SK0, y1[i] * SK1);
      const unsigned sk = pkbf(bflo(ub[i]) * d0, bfhi(ub[i]) * d1);
      const unsigned y2 = pkbf(bflo(yb) + bflo(sk), bfhi(yb) + bfhi(sk));
      hp[i] = pkbf(bflo(xc[i]) + bflo(y2), bfhi(xc[i]) + bfhi(y2));
    }
    const u32x2 h0 = {(hp[0] & 0xffffu) | (hp[1] << 16), (hp[2] & 0xffffu) | (hp[3] << 16)};
    const u32x2 h1 = {(hp[0] >> 16) | (hp[1] & 0xffff0000u), (hp[2] >> 16) | (hp[3] & 0xffff0000u)};
    auto rh = __builtin_amdgcn_make_buffer_rsrc((void*)(ws + WS_HT), 0, L * D * 2, 0x00020000);
    const int off = (tid >> 4) * (D * 128) + (2 * b) * 128 + (tid & 15) * 8;
    __builtin_amdgcn_raw_buffer_store_b64(h0, rh, off, 0, 16);
    __builtin_amdgcn_raw_buffer_store_b64(h1, rh, off + 128, 0, 16);
  }
}

constexpr int WIN0 = 14, WIN = 26;
constexpr int win_tgt(int t) { if (t < 0) return WIN0; int a = WIN0 + 3 * (t + 1), b = t + 1 + WIN; int m = a < b ? a : b; return m > 68 ? 68 : m; }
constexpr int M_HT = 0;
constexpr int M_ST = 57344;
constexpr int M_CS = M_ST + 2048;
constexpr int M_P = M_CS + 1024;
constexpr int M_LDS = M_P + 4 * 64 * 272;

__global__ __launch_bounds__(256, 1) void k_mlp(char* __restrict__ ws, unsigned short* __restrict__ out) {
  __shared__ __attribute__((aligned(16))) char smem_[M_LDS];
  LAS char* smem = (LAS char*)smem_;
  const int tid = threadIdx.x, lane = tid & 63;
  const int w = __builtin_amdgcn_readfirstlane(tid >> 6);
  const int xcd = blockIdx.x & 7, idx = blockIdx.x >> 3;
  const int rt = xcd * 4 + idx / 7, j = idx % 7;
  const u32x2 ev = *(const u32x2*)(ws + WS_EPOCH);
  auto rH = __builtin_amdgcn_make_buffer_rsrc((void*)(ws + WS_HT), 0, L * D * 2, 0x00020000);
  auto rW1 = __builtin_amdgcn_make_buffer_rsrc((void*)(ws + WS_W1F), 0, D * F * 2, 0x00020000);
  auto rW2 = __builtin_amdgcn_make_buffer_rsrc((void*)(ws + WS_W2F), 0, D * F * 2, 0x00020000);
  auto rH1 = __builtin_amdgcn_make_buffer_rsrc((void*)(ws + WS_H1F), 0, L * F * 2, 0x00020000);
  const int l16 = lane * 16;
  u32x4 hv[14];
#pragma unroll
  for (int p = 0; p < 14; ++p) hv[p] = __builtin_amdgcn_raw_buffer_load_b128(rH, tid * 16, rt * (D * 128) + p * 4096, 0);
  SB();
  float csp[4];
  {
    auto rC = __builtin_amdgcn_make_buffer_rsrc((void*)(ws + WS_CS), 0, 4 * F * 4, 0x00020000);
#pragma unroll
    for (int k = 0; k < 4; ++k) csp[k] = __uint_as_float(__builtin_amdgcn_raw_buffer_load_b32(rC, lane * 4, (k * F + 256 * j + 64 * w) * 4, 0));
  }
  const int ft0 = j * 8 + w * 2;
  u32x4 wa[56], wb[2][28];
  auto seqload = [&](int n) {
    if (n < 56) {
      const int a = n / 28, i = n % 28;
      const int ks = (((i / 7) + w) & 3) * 7 + (i % 7);
      wa[n] = __builtin_amdgcn_raw_buffer_load_b128(rW1, l16, ((ft0 + a) * 28 + ks) * 1024, 0);
    } else {
      const int m = n - 56, i = m >> 1, dt = m & 1;
      int jj = j + (i >> 2); jj = jj >= 7 ? jj - 7 : jj;
      const int fs = 16 * jj + 4 * w + (i & 3);
      wb[dt][i] = __builtin_amdgcn_raw_buffer_load_b128(rW2, l16, ((2 * j + dt) * 112 + fs) * 1024, 0);
    }
  };
#pragma unroll
  for (int n = 0; n < WIN0; ++n) seqload(n);
  SB();
  {
    const int r = tid >> 3, c = tid & 7;
#pragma unroll
    for (int p = 0; p < 14; ++p) { const int row = 32 * p + r; *(LAS u32x4*)(smem + M_HT + row * 128 + ((c ^ (4 * ((row >> 1) & 1))) * 16)) = hv[p]; }
  }
  *(LAS float*)(smem + M_CS + (w * 64 + lane) * 4) = (csp[0] + csp[1]) + (csp[2] + csp[3]);
  SB();
  BAR();
  const int hh = lane >> 5, blk = (lane >> 4) & 1, q = (lane & 15) >> 2, p = lane & 3;
  const int sw = (q >> 1) & 1;
  const LAS char* b0 = smem + M_HT + (8 * hh + q) * 128 + ((2 * blk + (p >> 1) + 4 * sw) * 16) + (p & 1) * 8;
  const LAS char* b1 = smem + M_HT + (8 * hh + q) * 128 + ((2 * blk + (p >> 1) + 4 * (1 - sw)) * 16) + (p & 1) * 8;
  f32x16 acc[2][2];
#pragma unroll
  for (int a = 0; a < 2; ++a)
#pragma unroll
    for (int c = 0; c < 2; ++c)
#pragma unroll
      for (int e = 0; e < 16; ++e) acc[a][c][e] = 0.f;
  float s0 = 0.f, s1 = 0.f, q0 = 0.f, q1 = 0.f;
  const unsigned ONES = 0x3f803f80u;
  constexpr int BD = 3;
  u32x4 bf[BD + 1][2];
  auto bload = [&](int i, u32x4& o0, u32x4& o1) {
    const int go = ((((i / 7) + w) & 3) * 7 + (i % 7)) * 2048;
    o0 = tr_read2(b0 + go, b0 + go + 512);
    o1 = tr_read2(b1 + go, b1 + go + 512);
  };
  float mr[2], ri[2];
  u32x4 own[2][4];
  auto epi = [&](int a, int c, int s) {
    u32x4 fr;
    const f32x4 c0 = *(const LAS f32x4*)(smem + M_CS + (w * 64 + a * 32 + 16 * s + 4 * hh) * 4);
    const f32x4 c1 = *(const LAS f32x4*)(smem + M_CS + (w * 64 + a * 32 + 16 * s + 8 + 4 * hh) * 4);
#pragma unroll
    for (int e = 0; e < 4; ++e) {
      const int r0 = 8 * s + 2 * e;
      const float k0 = e < 2 ? c0[2 * e] : c1[2 * e - 4], k1 = e < 2 ? c0[2 * e + 1] : c1[2 * e - 3];
      const float v0 = (acc[a][c][r0] - mr[c] * k0) * ri[c], v1 = (acc[a][c][r0 + 1] - mr[c] * k1) * ri[c];
      fr[e] = pkbf(fmaxf(v0, 0.f), fmaxf(v1, 0.f));
    }
    own[c][2 * a + s] = fr;
    const int fs = 16 * j + 4 * w + 2 * a + s;
    __builtin_amdgcn_raw_buffer_store_b128(fr, rH1, l16, ((rt * 2 + c) * 112 + fs) * 1024, 16);
  };
#pragma unroll
  for (int i = 0; i < BD; ++i) bload(i, bf[i][0], bf[i][1]);
  static_for<0, 56>([&](auto T_) {
    constexpr int t = decltype(T_)::value;
    constexpr int a = t / 28;
    if constexpr (t + BD < 56) bload((t + BD) % 28, bf[(t + BD) % (BD + 1)][0], bf[(t + BD) % (BD + 1)][1]);
    const u32x4 f0 = bf[t % (BD + 1)][0], f1 = bf[t % (BD + 1)][1];
    static_for<win_tgt(t - 1), win_tgt(t)>([&](auto N_) { seqload(decltype(N_)::value); });
    SB();
    acc[a][0] = mfma32b(wa[t], f0, acc[a][0]);
    acc[a][1] = mfma32b(wa[t], f1, acc[a][1]);
    if constexpr (t < 7) {
#pragma unroll
      for (int e = 0; e < 4; ++e) { s0 = dot2bf(f0[e], ONES, s0); q0 = dot2bf(f0[e], f0[e], q0); s1 = dot2bf(f1[e], ONES, s1); q1 = dot2bf(f1[e], f1[e], q1); }
    }
    if constexpr (t == 8) {
      s0 += __shfl_xor(s0, 32); s1 += __shfl_xor(s1, 32); q0 += __shfl_xor(q0, 32); q1 += __shfl_xor(q1, 32);
      if (hh == 0) {
        *(LAS u32x2*)(smem + M_ST + (w * 64 + lane) * 8) = u32x2{__float_as_uint(s0), __float_as_uint(q0)};
        *(LAS u32x2*)(smem + M_ST + (w * 64 + 32 + lane) * 8) = u32x2{__float_as_uint(s1), __float_as_uint(q1)};
      }
    }
    if constexpr (t == 27) {
      BAR();
#pragma unroll
      for (int c = 0; c < 2; ++c) {
        float s = 0.f, qq = 0.f;
#pragma unroll
        for (int ww = 0; ww < 4; ++ww) { u32x2 v = *(const LAS u32x2*)(smem + M_ST + (ww * 64 + c * 32 + (lane & 31)) * 8); s += __uint_as_float(v[0]); qq += __uint_as_float(v[1]); }
        const float mean = s / (float)D;
        const float var = fmaxf(qq / (float)D - mean * mean, 0.f);
        float mb, sb; ln_scalars(mean, var, mb, sb);
        mr[c] = mb; ri[c] = rcp_(sb);
      }
    }
    if constexpr (t >= 30 && t < 54 && (t - 30) % 6 == 0) { constexpr int e = (t - 30) / 6; epi(0, e >> 1, e & 1); }
    SB();
  });
#pragma unroll
  for (int e = 0; e < 4; ++e) { epi(1, e >> 1, e & 1); SB(); }
#pragma unroll
  for (int n = 68; n < 84; ++n) seqload(n);
  SB();
  unsigned elo = __builtin_amdgcn_readfirstlane(ev[0]), ehi = __builtin_amdgcn_readfirstlane(ev[1]);
  asm volatile("" : "+s"(elo), "+s"(ehi));
  const u64 tag = ((u64)ehi << 32) | elo;
  asm volatile("s_waitcnt vmcnt(16)" ::: "memory");
  GAS u64* flg = (GAS u64*)(ws + WS_FLAGB) + (rt * 4 + w) * 8;
  if (lane == 0) __hip_atomic_store(flg + j, tag, __ATOMIC_RELAXED, __HIP_MEMORY_SCOPE_AGENT);
  SB();
#pragma unroll
  for (int n = 84; n < 102; ++n) seqload(n);
  SB();
  const int fl = lane < 7 ? lane : 0;
  u64 fv = __hip_atomic_load(flg + fl, __ATOMIC_RELAXED, __HIP_MEMORY_SCOPE_AGENT);
  SB();
#pragma unroll
  for (int n = 102; n < 112; ++n) seqload(n);
  SB();
#pragma unroll
  for (int a = 0; a < 2; ++a)
#pragma unroll
    for (int c = 0; c < 2; ++c)
#pragma unroll
      for (int e = 0; e < 16; ++e) acc[a][c][e] = 0.f;
#pragma unroll
  for (int i = 0; i < 4; ++i) {
    acc[0][0] = mfma32b(wb[0][i], own[0][i], acc[0][0]);
    acc[0][1] = mfma32b(wb[0][i], own[1][i], acc[0][1]);
    acc[1][0] = mfma32b(wb[1][i], own[0][i], acc[1][0]);
    acc[1][1] = mfma32b(wb[1][i], own[1][i], acc[1][1]);
  }
  SB();
  {
    for (unsigned spins = 0;; ++spins) {
      if (__all(fv == tag)) break;
      if (spins > kSpinCap) { if (lane == 0) __hip_atomic_store((GAS unsigned*)(ws + WS_TMO), 2u, __ATOMIC_RELAXED, __HIP_MEMORY_SCOPE_AGENT); break; }
      fv = __hip_atomic_load(flg + fl, __ATOMIC_RELAXED, __HIP_MEMORY_SCOPE_AGENT);
    }
  }
  asm volatile("" ::: "memory");
  u32x4 hf[2][24];
  auto h1load = [&](int i) {
    const int n = i >> 2, fsub = i & 3;
    int jj = j + n; jj = jj >= 7 ? jj - 7 : jj;
    const int fs = 16 * jj + 4 * w + fsub;
    hf[0][i - 4] = __builtin_amdgcn_raw_buffer_load_b128(rH1, l16, ((rt * 2) * 112 + fs) * 1024, 16);
    hf[1][i - 4] = __builtin_amdgcn_raw_buffer_load_b128(rH1, l16, ((rt * 2 + 1) * 112 + fs) * 1024, 16);
  };
  constexpr int HW = 8;
#pragma unroll
  for (int i = 4; i < 4 + HW; ++i) h1load(i);
  SB();
  static_for<4, 28>([&](auto I_) {
    constexpr int i = decltype(I_)::value;
    if constexpr (i + HW < 28) h1load(i + HW);
    SB();
    acc[0][0] = mfma32b(wb[0][i], hf[0][i - 4], acc[0][0]);
    acc[0][1] = mfma32b(wb[0][i], hf[1][i - 4], acc[0][1]);
    acc[1][0] = mfma32b(wb[1][i], hf[0][i - 4], acc[1][0]);
    acc[1][1] = mfma32b(wb[1][i], hf[1][i - 4], acc[1][1]);
    SB();
  });
#pragma unroll
  for (int a = 0; a < 2; ++a)
#pragma unroll
    for (int c = 0; c < 2; ++c)
#pragma unroll
      for (int g4 = 0; g4 < 4; ++g4) {
        f32x4 v = {acc[a][c][4 * g4], acc[a][c][4 * g4 + 1], acc[a][c][4 * g4 + 2], acc[a][c][4 * g4 + 3]};
        *(LAS f32x4*)(smem + M_P + (w * 64 + c * 32 + (lane & 31)) * 272 + (a * 32 + 8 * g4 + 4 * hh) * 4) = v;
      }
  BAR();
  {
    const int l = lane, c4 = w;
    float o[16];
#pragma unroll
    for (int e = 0; e < 16; ++e) o[e] = 0.f;
#pragma unroll
    for (int ww = 0; ww < 4; ++ww)
#pragma unroll
      for (int i = 0; i < 4; ++i) {
        f32x4 v = *(const LAS f32x4*)(smem + M_P + (ww * 64 + l) * 272 + (16 * c4 + 4 * i) * 4);
#pragma unroll
        for (int e = 0; e < 4; ++e) o[4 * i + e] += v[e];
      }
    unsigned pk[8];
#pragma unroll
    for (int e = 0; e < 8; ++e) {
      const int r0 = 64 * j + 16 * c4 + 2 * e, r1 = r0 + 1;
      const float h0 = bf2f(*(const LAS unsigned short*)(smem + M_HT + r0 * 128 + (((l >> 3) ^ (4 * ((r0 >> 1) & 1))) * 16) + (l & 7) * 2));
      const float h1 = bf2f(*(const LAS unsigned short*)(smem + M_HT + r1 * 128 + (((l >> 3) ^ (4 * ((r1 >> 1) & 1))) * 16) + (l & 7) * 2));
      const unsigned ob = pkbf(o[2 * e], o[2 * e + 1]);
      pk[e] = pkbf(h0 + bflo(ob), h1 + bfhi(ob));
    }
    char* op = (char*)out + ((size_t)(rt * 64 + l) * D + 64 * j + 16 * c4) * 2;
    *(u32x4*)op = u32x4{pk[0], pk[1], pk[2], pk[3]};
    *(u32x4*)(op + 16) = u32x4{pk[4], pk[5], pk[6], pk[7]};
  }
}

}

extern "C" void kernel_launch(void* const* d_in, const int* in_sizes, int n_in, void* d_out, int out_size, void* d_ws, size_t ws_size, hipStream_t stream) {
  typedef const unsigned short* cp;
  k_ssm<<<256, 512, 0, stream>>>((cp)d_in[0], (cp)d_in[1], (cp)d_in[2], (cp)d_in[3], (cp)d_in[4], (cp)d_in[5], (cp)d_in[6], (cp)d_in[7], (cp)d_in[8], (cp)d_in[10], (cp)d_in[11], (char*)d_ws);
  k_mlp<<<224, 256, 0, stream>>>((char*)d_ws, (unsigned short*)d_out);
}
```

```cpp
#include <hip/hip_runtime.h>
#include <stdint.h>

namespace {

typedef __attribute__((ext_vector_type(8))) _Float16 f16x8;
typedef __attribute__((ext_vector_type(4))) _Float16 f16x4;
typedef __attribute__((ext_vector_type(2))) _Float16 f16x2;
typedef __attribute__((ext_vector_type(8))) __bf16 bf16x8;
typedef __attribute__((ext_vector_type(2))) __bf16 bf16x2;
typedef __attribute__((ext_vector_type(4))) float f32x4;
typedef __attribute__((ext_vector_type(16))) float f32x16;
typedef __attribute__((ext_vector_type(4))) unsigned u32x4;
typedef __attribute__((ext_vector_type(2))) unsigned u32x2;
typedef __attribute__((ext_vector_type(4))) short s16x4;
typedef unsigned long long u64;
#define LAS __attribute__((address_space(3)))
#define GAS __attribute__((address_space(1)))
#define DI __device__ __forceinline__

constexpr int L = 2048, D = 448, NM = 256, F = 1792;
#define CW_A 12

constexpr size_t WS_EPOCH = 0;
constexpr size_t WS_TMO = 64;
constexpr size_t WS_FLAGB = 8192;
constexpr size_t WS_STATS = 16384;
constexpr size_t WS_XT = WS_STATS + 1024 * 16;
constexpr size_t WS_CS = WS_XT + (size_t)512 * D * 16;
constexpr size_t WS_HT = WS_CS + 4 * F * 4;
constexpr size_t WS_W1F = WS_HT + (size_t)D * L * 2;
constexpr size_t WS_W2F = WS_W1F + (size_t)D * F * 2;
constexpr size_t WS_H1F = WS_W2F + (size_t)D * F * 2;
constexpr size_t WS_END = WS_H1F + (size_t)L * F * 2;
constexpr unsigned kSpinCap = 1u << 20;

#define SB() __builtin_amdgcn_sched_barrier(0)
#define BAR() do { asm volatile("s_waitcnt lgkmcnt(0)" ::: "memory"); __builtin_amdgcn_s_barrier(); asm volatile("" ::: "memory"); } while (0)
DI float bf2f(unsigned b) { return __uint_as_float(b << 16); }
DI float bflo(unsigned p) { return __uint_as_float(p << 16); }
DI float bfhi(unsigned p) { return __uint_as_float(p & 0xffff0000u); }
DI unsigned pkbf(float a, float b) { bf16x2 r; r[0] = (__bf16)a; r[1] = (__bf16)b; return __builtin_bit_cast(unsigned, r); }
DI float rbf(float a) { return bflo(pkbf(a, a)); }
DI unsigned pkh(float a, float b) { f16x2 r; r[0] = (_Float16)a; r[1] = (_Float16)b; return __builtin_bit_cast(unsigned, r); }

template <int CTRL> DI float dppf(float v) { return __int_as_float(__builtin_amdgcn_update_dpp(0, __float_as_int(v), CTRL, 0xF, 0xF, true)); }
DI float rdl(float v, int l) { return __int_as_float(__builtin_amdgcn_readlane(__float_as_int(v), l)); }
DI float wave_sum(float v) {
  v += dppf<0xB1>(v); v += dppf<0x4E>(v); v += dppf<0x141>(v); v += dppf<0x140>(v);
  return (rdl(v, 0) + rdl(v, 16)) + (rdl(v, 32) + rdl(v, 48));
}
DI float wave_max(float v) {
  v = fmaxf(v, dppf<0xB1>(v)); v = fmaxf(v, dppf<0x4E>(v)); v = fmaxf(v, dppf<0x141>(v)); v = fmaxf(v, dppf<0x140>(v));
  return fmaxf(fmaxf(rdl(v, 0), rdl(v, 16)), fmaxf(rdl(v, 32), rdl(v, 48)));
}
DI s16x4 tr_read(const LAS char* p) { return __builtin_amdgcn_ds_read_tr16_b64_v4i16((LAS s16x4*)p); }
DI u32x4 tr_read2(const LAS char* p0, const LAS char* p1) {
  s16x4 a = tr_read(p0), b = tr_read(p1);
  u32x2 a2 = __builtin_bit_cast(u32x2, a), b2 = __builtin_bit_cast(u32x2, b);
  u32x4 r; r[0] = a2[0]; r[1] = a2[1]; r[2] = b2[0]; r[3] = b2[1]; return r;
}
DI f32x4 mfma16h(u32x4 a, u32x4 b, f32x4 c) { return __builtin_amdgcn_mfma_f32_16x16x32_f16(__builtin_bit_cast(f16x8, a), __builtin_bit_cast(f16x8, b), c, 0, 0, 0); }
DI f32x16 mfma32b(u32x4 a, u32x4 b, f32x16 c) { return __builtin_amdgcn_mfma_f32_32x32x16_bf16(__builtin_bit_cast(bf16x8, a), __builtin_bit_cast(bf16x8, b), c, 0, 0, 0); }
DI float dot2bf(unsigned a, unsigned b, float c) { return __builtin_amdgcn_fdot2_f32_bf16(__builtin_bit_cast(bf16x2, a), __builtin_bit_cast(bf16x2, b), c, false); }

template <int I> struct IC { static constexpr int value = I; };
template <int B, int E, class Fn> DI void static_for(Fn&& fn) { if constexpr (B < E) { fn(IC<B>{}); static_for<B + 1, E>(fn); } }
template <int NKB> DI void code_warm_issue(u32x4 (&v)[NKB], int lane, bool on) {
  const char* pc = (const char*)(__builtin_amdgcn_s_getpc() & ~(unsigned long long)63);
#pragma unroll
  for (int i = 0; i < NKB; ++i) v[i] = u32x4{0u, 0u, 0u, 0u};
  if (on) {
#pragma unroll
    for (int i = 0; i < NKB; ++i) v[i] = *(const u32x4*)(pc + i * 1024 + lane * 16);
  }
}
template <int NKB> DI void code_warm_retire(u32x4 (&v)[NKB]) {
#pragma unroll
  for (int i = 0; i < NKB; ++i) asm volatile("" :: "v"(v[i]));
}
struct cf { float r, i; };
DI cf cmul(cf a, cf b) { cf o; o.r = a.r * b.r - a.i * b.i; o.i = a.r * b.i + a.i * b.r; return o; }
DI cf csq(cf a) { cf o; o.r = a.r * a.r - a.i * a.i; o.i = 2.f * a.r * a.i; return o; }
DI cf cexp_p2(float zr2, float zf, float p2) {
  float m = __builtin_amdgcn_exp2f(zr2 * p2);
  float t = __builtin_amdgcn_fractf(zf * p2);
  cf o; o.r = m * __builtin_amdgcn_cosf(t); o.i = m * __builtin_amdgcn_sinf(t); return o;
}

DI void ln_scalars(float mean, float var, float& mb, float& sb) {
  mb = rbf(mean);
  float vb = rbf(var);
  sb = rbf(__builtin_sqrtf(rbf(vb + 1e-5f)));
}
DI float rcp_(float s) { return __builtin_amdgcn_rcpf(s); }

struct IdxTab { unsigned short v[256][12]; };
constexpr int bf16_round_index(int lag) {
  if (lag < 0 || lag > 2047) return 641;
  if (lag < 256) return lag;
  int e = 8; while ((lag >> (e + 1)) != 0) ++e;
  const int sh = e - 7;
  int q = lag >> sh; const int r = lag & ((1 << sh) - 1), half = 1 << (sh - 1);
  if (r > half || (r == half && (q & 1))) ++q;
  return 256 + (e - 8) * 128 + (q - 128);
}
constexpr IdxTab make_idx() {
  IdxTab t{};
  for (int c = 0; c < 256; ++c) { for (int e = 0; e < 11; ++e) t.v[c][e] = (unsigned short)(2 * bf16_round_index(2048 - 8 * c + 3 - e)); t.v[c][11] = 0; }
  return t;
}
__device__ const IdxTab g_idx = make_idx();

constexpr int A_TAB = 0;
constexpr int A_MISC = A_TAB + 8 * 16384;
constexpr int A_KDC = A_MISC;
constexpr int A_SC = A_KDC + 2 * 1312;
constexpr int A_XS = A_SC + 128;
constexpr int A_LDS = A_XS + 9216 + 64;
constexpr int TOE_CP = 4160, TOE_CH = 4 * TOE_CP;
constexpr int A_U16 = A_TAB + 4096;
constexpr int A_TOE2 = A_TAB + 13312;
constexpr int A_YP = A_TAB + 46592;

__global__ __launch_bounds__(512, 2) void k_ssm(const unsigned short* __restrict__ x, const unsigned short* __restrict__ Are, const unsigned short* __restrict__ Aim,
                                                 const unsigned short* __restrict__ Bre, const unsigned short* __restrict__ Bim, const unsigned short* __restrict__ Cre,
                                                 const unsigned short* __restrict__ Cim, const unsigned short* __restrict__ logdt, const unsigned short* __restrict__ Dsk,
                                                 const unsigned short* __restrict__ w1, const unsigned short* __restrict__ w2, char* __restrict__ ws) {
  __shared__ __attribute__((aligned(16))) char smem_[A_LDS];
  LAS char* smem = (LAS char*)smem_;
  const int tid = threadIdx.x, lane = tid & 63;
  const int w = __builtin_amdgcn_readfirstlane(tid >> 6);
  const int b = blockIdx.x;
  const bool has_ch = b < 224;
  const int c2 = w >> 2, w4 = w & 3;
  const int ch = 2 * b + c2;
  u32x4 cw[CW_A];
  code_warm_issue<CW_A>(cw, lane, w == 7);
  const int bs = has_ch ? b : 0;
  const int chs = 2 * bs + c2;
  const int po = chs * NM + w4 * 64 + lane;
  const unsigned short r_are = Are[po], r_aim = Aim[po], r_bre = Bre[po], r_bim = Bim[po], r_cre = Cre[po], r_cim = Cim[po];
  const unsigned short r_ldt = logdt[chs];
  const u32x2 ev = *(const u32x2*)(ws + WS_EPOCH);
  SB();
  const int row = 8 * b + w;
  u32x4 xv = *(const u32x4*)(x + (size_t)row * D + min(lane, 55) * 8);
  const int trt = bs & 31, tg = bs >> 5;
  const u32x4 xt = *(const u32x4*)(x + (size_t)(64 * trt + 8 * w + (lane >> 3)) * D + 64 * tg + (lane & 7) * 8);
  const unsigned r_dsk = *(const unsigned*)(Dsk + 2 * bs);
  const int t = tid & 255;
  const u32x4 ix0 = *(const u32x4*)&g_idx.v[t][0];
  const u32x2 ix1 = *(const u32x2*)&g_idx.v[t][8];
  SB();
  unsigned elo = __builtin_amdgcn_readfirstlane(ev[0]), ehi = __builtin_amdgcn_readfirstlane(ev[1]);
  asm volatile("" : "+s"(elo), "+s"(ehi));
  const u64 tag = (((u64)ehi << 32) | elo) + 1;
  const unsigned tlo_ = (unsigned)tag, thi_ = (unsigned)(tag >> 32);
  LAS char* tab = smem + A_TAB + w * 16384;

  auto ln_row = [&]() {
    if (lane >= 56) xv = u32x4{0u, 0u, 0u, 0u};
    float xf[8];
#pragma unroll
    for (int j = 0; j < 4; ++j) { xf[2 * j] = bflo(xv[j]); xf[2 * j + 1] = bfhi(xv[j]); }
    float s = 0.f;
#pragma unroll
    for (int j = 0; j < 8; ++j) s += xf[j];
    const float mean = wave_sum(s) / (float)D;
    float q = 0.f;
#pragma unroll
    for (int j = 0; j < 8; ++j) { float d = xf[j] - mean; q += d * d; }
    if (lane >= 56) q = 0.f;
    const float var = wave_sum(q) / (float)D;
    float mb, sb; ln_scalars(mean, var, mb, sb);
    if (lane == 0) *(LAS unsigned*)(smem + A_XS + 9216 + w * 4) = (__float_as_uint(mb) >> 16) | (__float_as_uint(sb) & 0xffff0000u);
    if (has_ch) *(LAS u32x4*)(smem + A_XS + (8 * w + (lane >> 3)) * 144 + (lane & 7) * 16) = xt;
  };
  auto ux_publish = [&]() {
    if (tid < 4) {
      const u32x2 p = *(const LAS u32x2*)(smem + A_XS + 9216 + tid * 8);
      auto rs = __builtin_amdgcn_make_buffer_rsrc((void*)(ws + WS_STATS), 0, 1024 * 16, 0x00020000);
      __builtin_amdgcn_raw_buffer_store_b128(u32x4{p[0], p[1], tlo_, thi_}, rs, (4 * b + tid) * 16, 0, 16);
    }
    if (has_ch) {
      auto rx = __builtin_amdgcn_make_buffer_rsrc((void*)(ws + WS_XT), 0, 512 * D * 16, 0x00020000);
      const int qd = lane & 15;
#pragma unroll
      for (int i = 0; i < 2; ++i) {
        const int c = 8 * w + 4 * i + (lane >> 4);
        const LAS char* a0 = smem + A_XS + (4 * qd) * 144 + c * 2;
        const unsigned r0 = *(const LAS unsigned short*)a0, r1 = *(const LAS unsigned short*)(a0 + 144), r2 = *(const LAS unsigned short*)(a0 + 288), r3 = *(const LAS unsigned short*)(a0 + 432);
        __builtin_amdgcn_raw_buffer_store_b128(u32x4{r0 | (r1 << 16), r2 | (r3 << 16), tlo_, thi_}, rx, ((64 * tg + c) * 512 + 16 * trt + qd) * 16, 0, 16);
      }
    }
  };
  if (!has_ch) {
    u32x4 cm[2];
    {
      unsigned long long pa;
      asm volatile("s_getpc_b64 s[40:41]\n\ts_add_u32 s40, s40, _ZN12_GLOBAL__N_15k_mlpEPcPt@rel32@lo+4\n\ts_addc_u32 s41, s41, _ZN12_GLOBAL__N_15k_mlpEPcPt@rel32@hi+12\n\ts_mov_b64 %0, s[40:41]" : "=s"(pa) :: "s40", "s41", "scc");
      cm[0] = *(const u32x4*)((const char*)pa + w * 1024 + lane * 16);
      cm[1] = *(const u32x4*)((const char*)pa + (w < 4 ? (8 + w) * 1024 : w * 1024) + lane * 16);
    }
    SB();
    ln_row();
    BAR();
    ux_publish();
    code_warm_retire<CW_A>(cw);
    asm volatile("" :: "v"(cm[0]), "v"(cm[1]));
    const int task = (b - 224) * 8 + w;
    if (task < 224) {
      const bool is_w1 = task < 112;
      const int tk = is_w1 ? task : task - 112;
      const int nb = is_w1 ? tk % 28 : tk % 7, kg = is_w1 ? tk / 28 : tk / 7;
      const int rld = is_w1 ? F : D;
      const unsigned short* src = (is_w1 ? w1 : w2) + (size_t)(16 * 7 * kg + (lane >> 3)) * rld + 64 * nb + (lane & 7) * 8;
      u32x4 r0[7], r1[7];
#pragma unroll
      for (int u = 0; u < 7; ++u) { r0[u] = *(const u32x4*)(src + (size_t)(16 * u) * rld); r1[u] = *(const u32x4*)(src + (size_t)(16 * u + 8) * rld); }
      LAS char* sc = smem + w * 16384;
#pragma unroll
      for (int u = 0; u < 7; ++u) { *(LAS u32x4*)(sc + u * 2048 + lane * 16) = r0[u]; *(LAS u32x4*)(sc + u * 2048 + 1024 + lane * 16) = r1[u]; }
      __builtin_amdgcn_s_waitcnt(0xC07F);
      __builtin_amdgcn_wave_barrier();
      const int g = lane >> 4, blk = g & 1, hh = g >> 1, q = (lane & 15) >> 2, p = lane & 3;
      const int q0 = is_w1 ? (8 * hh + q) : (4 * hh + q);
      const int q1 = is_w1 ? (8 * hh + 4 + q) : (8 + 4 * hh + q);
      auto rd = __builtin_amdgcn_make_buffer_rsrc((void*)(ws + (is_w1 ? WS_W1F : WS_W2F)), 0, D * F * 2, 0x00020000);
      const int nks = is_w1 ? 28 : 112;
      float cs[2] = {0.f, 0.f};
#pragma unroll
      for (int u = 0; u < 7; ++u) {
#pragma unroll
        for (int tt = 0; tt < 2; ++tt) {
          const int cb = (tt * 32 + blk * 16 + 4 * p) * 2;
          u32x4 fr = tr_read2(sc + u * 2048 + q0 * 128 + cb, sc + u * 2048 + q1 * 128 + cb);
          __builtin_amdgcn_raw_buffer_store_b128(fr, rd, lane * 16, ((2 * nb + tt) * nks + 7 * kg + u) * 1024, 16);
#pragma unroll
          for (int e = 0; e < 4; ++e) cs[tt] = dot2bf(fr[e], 0x3f803f80u, cs[tt]);
        }
      }
      if (is_w1) {
        cs[0] += __shfl_xor(cs[0], 32); cs[1] += __shfl_xor(cs[1], 32);
        auto rc = __builtin_amdgcn_make_buffer_rsrc((void*)(ws + WS_CS), 0, 4 * F * 4, 0x00020000);
        __builtin_amdgcn_raw_buffer_store_b32(__float_as_uint(lane < 32 ? cs[0] : cs[1]), rc, (kg * F + 64 * nb + lane) * 4, 0, 16);
      }
    }
    return;
  }
  float k2048;
  int ew;
  {
    const float are = bf2f(r_are), aim = bf2f(r_aim), bre = bf2f(r_bre), bim = bf2f(r_bim), cre = bf2f(r_cre), cim = bf2f(r_cim);
    const float dt = rbf(expf(bf2f(r_ldt)));
    const float zr = dt * are, zi = dt * aim;
    const float zr2 = zr * 1.44269504088896341f, zf = zi * 0.15915494309189535f;
    const float dbr = dt * bre, dbi = dt * bim;
    cf CB; CB.r = cre * dbr - cim * dbi; CB.i = cre * dbi + cim * dbr;
    const float mx = wave_max(fmaxf(fabsf(CB.r), fabsf(CB.i)));
    const float gz = fmaxf(wave_max(zr2), 0.f) * 2048.f;
    int e = (mx > 0.f) ? ((int)(__float_as_uint(mx) >> 23) - 127) : 0;
    e += (int)ceilf(fminf(gz, 100.f));
    e = max(-120, min(120, e));
    ew = e;
    const float S = __uint_as_float((unsigned)(127 - e) << 23);
    cf G0; G0.r = CB.r * S; G0.i = CB.i * S;
    const cf E1 = cexp_p2(zr2, zf, 1.f), E2 = csq(E1), E4 = cexp_p2(zr2, zf, 4.f), E8 = csq(E4);
    const cf E16 = cexp_p2(zr2, zf, 16.f), E32 = csq(E16), E64 = cexp_p2(zr2, zf, 64.f), E128 = csq(E64);
    const cf E256 = cexp_p2(zr2, zf, 256.f), E512 = csq(E256), E1024 = cexp_p2(zr2, zf, 1024.f), E2048 = csq(E1024);
    SB();
    ln_row();
    SB();
    const int sw = (lane >> 1) & 3;
    LAS char* tb = tab + lane * 64;
    auto put = [&](int tile, int half, const cf* v, float isign) {
      u32x4 re, im;
#pragma unroll
      for (int j = 0; j < 4; ++j) { re[j] = pkh(v[2 * j].r, v[2 * j + 1].r); im[j] = pkh(isign * v[2 * j].i, isign * v[2 * j + 1].i); }
      *(LAS u32x4*)(tb + tile * 4096 + ((half ^ sw) * 16)) = re;
      *(LAS u32x4*)(tb + tile * 4096 + (((2 + half) ^ sw) * 16)) = im;
    };
    cf Er[8];
    Er[0].r = 1.f; Er[0].i = 0.f; Er[1] = E1; Er[2] = E2; Er[3] = cmul(E1, E2); Er[4] = E4; Er[5] = cmul(E4, E1); Er[6] = cmul(E4, E2); Er[7] = cmul(E4, Er[3]);
    {
      cf Y[8];
      Y[0].r = 1.f; Y[0].i = 0.f; Y[1] = E8; Y[2] = E16; Y[3] = cmul(E8, E16); Y[4] = E32; Y[5] = cmul(E32, E8); Y[6] = cmul(E32, E16); Y[7] = cmul(E32, Y[3]);
      put(3, 0, Y, 1.f);
      cf Y2[8];
      Y2[0] = E64;
#pragma unroll
      for (int j = 1; j < 8; ++j) Y2[j] = cmul(Y[j], E64);
      put(3, 1, Y2, 1.f);
    }
    cf G[8];
    G[0] = G0; G[1] = cmul(G0, E128); G[2] = cmul(G0, E256); G[3] = cmul(G[1], E256);
#pragma unroll
    for (int j = 0; j < 4; ++j) G[4 + j] = cmul(G[j], E512);
    {
      cf T[8];
      T[0] = G[0];
#pragma unroll
      for (int j = 1; j < 8; ++j) T[j] = cmul(G[0], Er[j]);
      put(0, 0, T, -1.f);
      SB();
      BAR();
      ux_publish();
      SB();
      T[0] = G[1];
#pragma unroll
      for (int j = 1; j < 8; ++j) T[j] = cmul(G[1], Er[j]);
      put(0, 1, T, -1.f);
    }
    {
      cf T[8];
      T[0] = G[2]; T[1] = cmul(G[2], E2); T[2] = cmul(G[2], E4); T[3] = cmul(G[2], Er[6]);
      T[4] = G[3]; T[5] = cmul(G[3], E2); T[6] = cmul(G[3], E4); T[7] = cmul(G[3], Er[6]);
      put(1, 0, T, -1.f);
#pragma unroll
      for (int j = 0; j < 4; ++j) { T[2 * j] = G[4 + j]; T[2 * j + 1] = cmul(G[4 + j], E4); }
      put(1, 1, T, -1.f);
    }
    {
      cf T[8];
#pragma unroll
      for (int j = 0; j < 8; ++j) T[j] = cmul(G[j], E1024);
      put(2, 0, T, -1.f);
    }
    k2048 = wave_sum(G0.r * E2048.r - G0.i * E2048.i);
  }
  __builtin_amdgcn_s_waitcnt(0xC07F);
  __builtin_amdgcn_wave_barrier();
  {
    const int g = lane >> 4, q = (lane & 15) >> 2, p = lane & 3;
    f32x4 acc0 = {0.f, 0.f, 0.f, 0.f}, acc1 = acc0, acc2 = acc0;
    const int part = q & 1, cl = part * 2 + (p >> 1);
    u32x4 bb[4], a0[4], a1[4], a2[4];
#pragma unroll
    for (int s = 0; s < 4; ++s) {
      const int m0 = 16 * s + 8 * (g >> 1) + 2 * (g & 1) + (q >> 1), m1 = m0 + 4;
      const int o0 = m0 * 64 + ((cl ^ ((m0 >> 1) & 3)) * 16) + (p & 1) * 8;
      const int o1 = m1 * 64 + ((cl ^ ((m1 >> 1) & 3)) * 16) + (p & 1) * 8;
      bb[s] = tr_read2(tab + 3 * 4096 + o0, tab + 3 * 4096 + o1);
      a0[s] = tr_read2(tab + o0, tab + o1);
      a1[s] = tr_read2(tab + 4096 + o0, tab + 4096 + o1);
      a2[s] = tr_read2(tab + 8192 + o0, tab + 8192 + o1);
    }
    SB();
#pragma unroll
    for (int s = 0; s < 4; ++s) { acc0 = mfma16h(a0[s], bb[s], acc0); acc1 = mfma16h(a1[s], bb[s], acc1); acc2 = mfma16h(a2[s], bb[s], acc2); }
    SB();
    *(LAS f32x4*)(tab + lane * 16) = acc0;
    *(LAS f32x4*)(tab + 1024 + lane * 16) = acc1;
    *(LAS f32x4*)(tab + 2048 + lane * 16) = acc2;
    if (lane == 0) { *(LAS int*)(smem + A_SC + w * 8) = ew; *(LAS float*)(smem + A_SC + w * 8 + 4) = k2048; }
  }
  auto rst = __builtin_amdgcn_make_buffer_rsrc((void*)(ws + WS_STATS), 0, 1024 * 16, 0x00020000);
  auto rxq = __builtin_amdgcn_make_buffer_rsrc((void*)(ws + WS_XT), 0, 512 * D * 16, 0x00020000);
  u32x4 gr[4];
  auto sweep = [&]() {
    gr[0] = __builtin_amdgcn_raw_buffer_load_b128(rst, tid * 32, 0, 16);
    gr[1] = __builtin_amdgcn_raw_buffer_load_b128(rst, tid * 32 + 16, 0, 16);
    gr[2] = __builtin_amdgcn_raw_buffer_load_b128(rxq, tid * 16, (2 * b) * 8192, 16);
    gr[3] = __builtin_amdgcn_raw_buffer_load_b128(rxq, tid * 16, (2 * b + 1) * 8192, 16);
  };
  sweep();
  BAR();
  float SKinv;
  {
    int e4[4]; float k4[4];
#pragma unroll
    for (int j = 0; j < 4; ++j) { e4[j] = *(LAS int*)(smem + A_SC + (c2 * 4 + j) * 8); k4[j] = *(LAS float*)(smem + A_SC + (c2 * 4 + j) * 8 + 4); }
    const int em = max(max(e4[0], e4[1]), max(e4[2], e4[3]));
    float fw[4];
#pragma unroll
    for (int j = 0; j < 4; ++j) { int d = max(e4[j] - em + 5, -120); fw[j] = __uint_as_float((unsigned)(127 + d) << 23); }
    SKinv = __uint_as_float((unsigned)(127 + em - 5) << 23);
    LAS char* kdc = smem + A_KDC + c2 * 1312;
    if (t < 192) {
      const int tile = t >> 6, ln = t & 63, c = ln & 15, g = ln >> 4;
      f32x4 s = {0.f, 0.f, 0.f, 0.f};
#pragma unroll
      for (int j = 0; j < 4; ++j) { f32x4 v = *(LAS f32x4*)(smem + A_TAB + (c2 * 4 + j) * 16384 + tile * 1024 + ln * 16); s += v * fw[j]; }
      const unsigned p01 = pkh(s[0], s[1]), p23 = pkh(s[2], s[3]);
      if (tile == 0) {
        u32x2 v = {p01, p23}; *(LAS u32x2*)(kdc + (128 * (g >> 1) + 8 * c + 4 * (g & 1)) * 2) = v;
      } else if (tile == 1) {
        if (g < 2) { u32x2 v = {p01, p23}; *(LAS u32x2*)(kdc + (256 + 64 * g + 4 * c) * 2) = v; }
        else { const int i0 = 384 + 64 * (g - 2) + 2 * c; *(LAS unsigned*)(kdc + i0 * 2) = p01; *(LAS unsigned*)(kdc + (i0 + 32) * 2) = p23; }
      } else if (g < 2) {
        const int i0 = 512 + 64 * g + c;
        *(LAS unsigned short*)(kdc + i0 * 2) = (unsigned short)p01; *(LAS unsigned short*)(kdc + (i0 + 16) * 2) = (unsigned short)(p01 >> 16);
        *(LAS unsigned short*)(kdc + (i0 + 32) * 2) = (unsigned short)p23; *(LAS unsigned short*)(kdc + (i0 + 48) * 2) = (unsigned short)(p23 >> 16);
      }
    } else if (t == 192) {
      const float k = k4[0] * fw[0] + k4[1] * fw[1] + k4[2] * fw[2] + k4[3] * fw[3];
      *(LAS unsigned*)(kdc + 640 * 2) = pkh(k, 0.f);
    }
  }
  float SKo;
  {
    int e4[4];
#pragma unroll
    for (int j = 0; j < 4; ++j) e4[j] = *(LAS int*)(smem + A_SC + ((1 - c2) * 4 + j) * 8);
    const int em = max(max(e4[0], e4[1]), max(e4[2], e4[3]));
    SKo = __uint_as_float((unsigned)(127 + em - 5) << 23);
  }
  const float SK0 = c2 ? SKo : SKinv, SK1 = c2 ? SKinv : SKo;
  unsigned ub[4], xc[4];
  {
    for (unsigned spins = 0;; ++spins) {
      bool ok = true;
#pragma unroll
      for (int i = 0; i < 4; ++i) ok = ok && gr[i][2] == tlo_ && gr[i][3] == thi_;
      if (__all(ok)) break;
      if (spins > kSpinCap) { if (lane == 0) __hip_atomic_store((GAS unsigned*)(ws + WS_TMO), 1u, __ATOMIC_RELAXED, __HIP_MEMORY_SCOPE_AGENT); break; }
      sweep();
    }
    xc[0] = (gr[2][0] & 0xffffu) | (gr[3][0] << 16); xc[1] = (gr[2][0] >> 16) | (gr[3][0] & 0xffff0000u);
    xc[2] = (gr[2][1] & 0xffffu) | (gr[3][1] << 16); xc[3] = (gr[2][1] >> 16) | (gr[3][1] & 0xffff0000u);
    float u0[4], u1[4];
#pragma unroll
    for (int i = 0; i < 4; ++i) {
      const unsigned pay = gr[i >> 1][i & 1];
      const float mb = bflo(pay), ri = rcp_(bfhi(pay));
      const unsigned tt = pkbf(bflo(xc[i]) - mb, bfhi(xc[i]) - mb);
      ub[i] = pkbf(bflo(tt) * ri, bfhi(tt) * ri);
      u0[i] = bflo(ub[i]); u1[i] = bfhi(ub[i]);
    }
    *(LAS u32x2*)(smem + A_U16 + 512 + tid * 8) = u32x2{pkh(u0[0], u0[1]), pkh(u0[2], u0[3])};
    *(LAS u32x2*)(smem + A_U16 + 4608 + 512 + tid * 8) = u32x2{pkh(u1[0], u1[1]), pkh(u1[2], u1[3])};
    if (t < 32) *(LAS u32x4*)(smem + A_U16 + c2 * 4608 + t * 16) = u32x4{0u, 0u, 0u, 0u};
  }
  code_warm_retire<CW_A>(cw);
  BAR();
  u32x4 Bf[2][8];
  {
    const int bq = lane & 15, g = lane >> 4;
    const LAS char* ua = smem + A_U16 + c2 * 4608 + (256 + 16 * (bq - 1) + 8 * g) * 2 - 64 * w4 - 256;
#pragma unroll
    for (int it = 0; it < 2; ++it)
#pragma unroll
      for (int k = 0; k < 8; ++k) Bf[it][k] = *(const LAS u32x4*)(ua + k * 512 + (1 - it) * 256);
  }
  {
    const LAS char* kdc = smem + A_KDC + c2 * 1312;
    LAS char* toe = smem + A_TOE2 + c2 * TOE_CH;
    unsigned v[11];
#pragma unroll
    for (int e = 0; e < 11; ++e) {
      const unsigned wd = e < 8 ? ix0[e >> 1] : ix1[(e - 8) >> 1];
      const unsigned off = (e & 1) ? (wd >> 16) : (wd & 0xffffu);
      v[e] = *(const LAS unsigned short*)(kdc + off);
    }
    unsigned ev_[5], od[5];
#pragma unroll
    for (int j = 0; j < 5; ++j) { ev_[j] = v[2 * j] | (v[2 * j + 1] << 16); od[j] = v[2 * j + 1] | (v[2 * j + 2] << 16); }
    *(LAS u32x4*)(toe + 0 * TOE_CP + t * 16) = u32x4{od[1], od[2], od[3], od[4]};
    *(LAS u32x4*)(toe + 1 * TOE_CP + t * 16) = u32x4{ev_[1], ev_[2], ev_[3], ev_[4]};
    *(LAS u32x4*)(toe + 2 * TOE_CP + t * 16) = u32x4{od[0], od[1], od[2], od[3]};
    *(LAS u32x4*)(toe + 3 * TOE_CP + t * 16) = u32x4{ev_[0], ev_[1], ev_[2], ev_[3]};
    if (t < 12) {
      const int s = t & 3, c = 256 + (t >> 2);
      const u32x2 k03 = *(const LAS u32x2*)kdc;
      const unsigned k0 = k03[0] & 0xffffu, k1 = k03[0] >> 16, k2 = k03[1] & 0xffffu, k3 = k03[1] >> 16;
      unsigned d0 = s == 0 ? k0 : s == 1 ? (k1 | (k0 << 16)) : s == 2 ? (k2 | (k1 << 16)) : (k3 | (k2 << 16));
      unsigned d1 = s == 2 ? k0 : s == 3 ? (k1 | (k0 << 16)) : 0u;
      if (c != 256) { d0 = 0u; d1 = 0u; }
      *(LAS u32x4*)(toe + s * TOE_CP + c * 16) = u32x4{d0, d1, 0u, 0u};
    }
  }
  BAR();
  if (b == 0 && tid == 0) __hip_atomic_store((GAS u64*)(ws + WS_EPOCH), tag, __ATOMIC_RELAXED, __HIP_MEMORY_SCOPE_AGENT);
  {
    const int bq = lane & 15, g = lane >> 4;
    f32x4 acc[8];
#pragma unroll
    for (int i = 0; i < 8; ++i) acc[i] = f32x4{0.f, 0.f, 0.f, 0.f};
    const LAS char* ta = smem + A_TOE2 + c2 * TOE_CH + (bq & 3) * TOE_CP + (2032 - 4 * (bq >> 2) + 8 * g) * 2 - 64 * w4 - 512 * 7 - 256;
    u32x4 A[2][8];
#pragma unroll
    for (int it = 0; it < 2; ++it)
#pragma unroll
      for (int k = 0; k < 8; ++k) {
        const u32x2 lo = *(const volatile LAS u32x2*)(ta + (7 - k) * 512 + (1 - it) * 256), hi = *(const volatile LAS u32x2*)(ta + (7 - k) * 512 + (1 - it) * 256 + 8);
        A[it][k] = u32x4{lo[0], lo[1], hi[0], hi[1]};
      }
    SB();
#pragma unroll
    for (int it = 0; it < 2; ++it) {
#pragma unroll
      for (int k = 0; k < 8; ++k)
#pragma unroll
        for (int i = k; i < 8; ++i) acc[i] = mfma16h(A[it][k], Bf[it][i - k], acc[i]);
      SB();
    }
    LAS char* yp = smem + A_YP + w * 8192;
#pragma unroll
    for (int i = 0; i < 8; ++i) *(LAS f32x4*)(yp + i * 1024 + bq * 64 + g * 16) = acc[i];
  }
  BAR();
  {
    f32x4 y0 = {0.f, 0.f, 0.f, 0.f}, y1 = y0;
#pragma unroll
    for (int j = 0; j < 4; ++j) {
      y0 += *(const LAS f32x4*)(smem + A_YP + j * 8192 + tid * 16);
      y1 += *(const LAS f32x4*)(smem + A_YP + (4 + j) * 8192 + tid * 16);
    }
    const float d0 = bflo(r_dsk), d1 = bfhi(r_dsk);
    unsigned hp[4];
#pragma unroll
    for (int i = 0; i < 4; ++i) {
      const unsigned yb = pkbf(y0[i] * SK0, y1[i] * SK1);
      const unsigned sk = pkbf(bflo(ub[i]) * d0, bfhi(ub[i]) * d1);
      const unsigned y2 = pkbf(bflo(yb) + bflo(sk), bfhi(yb) + bfhi(sk));
      hp[i] = pkbf(bflo(xc[i]) + bflo(y2), bfhi(xc[i]) + bfhi(y2));
    }
    const u32x2 h0 = {(hp[0] & 0xffffu) | (hp[1] << 16), (hp[2] & 0xffffu) | (hp[3] << 16)};
    const u32x2 h1 = {(hp[0] >> 16) | (hp[1] & 0xffff0000u), (hp[2] >> 16) | (hp[3] & 0xffff0000u)};
    auto rh = __builtin_amdgcn_make_buffer_rsrc((void*)(ws + WS_HT), 0, L * D * 2, 0x00020000);
    const int off = (tid >> 4) * (D * 128) + (2 * b) * 128 + (tid & 15) * 8;
    __builtin_amdgcn_raw_buffer_store_b64(h0, rh, off, 0, 16);
    __builtin_amdgcn_raw_buffer_store_b64(h1, rh, off + 128, 0, 16);
  }
}

constexpr int WIN0 = 14, WIN = 26;
constexpr int win_tgt(int t) { if (t < 0) return WIN0; int a = WIN0 + 3 * (t + 1), b = t + 1 + WIN; int m = a < b ? a : b; return m > 68 ? 68 : m; }
constexpr int M_HT = 0;
constexpr int M_ST = 57344;
constexpr int M_CS = M_ST + 2048;
constexpr int M_P = M_CS + 1024;
constexpr int M_LDS = M_P + 4 * 64 * 272;

__global__ __launch_bounds__(256, 1) void k_mlp(char* __restrict__ ws, unsigned short* __restrict__ out) {
  __shared__ __attribute__((aligned(16))) char smem_[M_LDS];
  LAS char* smem = (LAS char*)smem_;
  const int tid = threadIdx.x, lane = tid & 63;
  const int w = __builtin_amdgcn_readfirstlane(tid >> 6);
  const int xcd = blockIdx.x & 7, idx = blockIdx.x >> 3;
  const int rt = xcd * 4 + idx / 7, j = idx % 7;
  const u32x2 ev = *(const u32x2*)(ws + WS_EPOCH);
  auto rH = __builtin_amdgcn_make_buffer_rsrc((void*)(ws + WS_HT), 0, L * D * 2, 0x00020000);
  auto rW1 = __builtin_amdgcn_make_buffer_rsrc((void*)(ws + WS_W1F), 0, D * F * 2, 0x00020000);
  auto rW2 = __builtin_amdgcn_make_buffer_rsrc((void*)(ws + WS_W2F), 0, D * F * 2, 0x00020000);
  auto rH1 = __builtin_amdgcn_make_buffer_rsrc((void*)(ws + WS_H1F), 0, L * F * 2, 0x00020000);
  const int l16 = lane * 16;
  u32x4 hv[14];
#pragma unroll
  for (int p = 0; p < 14; ++p) hv[p] = __builtin_amdgcn_raw_buffer_load_b128(rH, tid * 16, rt * (D * 128) + p * 4096, 0);
  SB();
  float csp[4];
  {
    auto rC = __builtin_amdgcn_make_buffer_rsrc((void*)(ws + WS_CS), 0, 4 * F * 4, 0x00020000);
#pragma unroll
    for (int k = 0; k < 4; ++k) csp[k] = __uint_as_float(__builtin_amdgcn_raw_buffer_load_b32(rC, lane * 4, (k * F + 256 * j + 64 * w) * 4, 0));
  }
  const int ft0 = j * 8 + w * 2;
  u32x4 wa[56], wb[2][28];
  auto seqload = [&](int n) {
    if (n < 56) {
      const int a = n / 28, i = n % 28;
      const int ks = (((i / 7) + w) & 3) * 7 + (i % 7);
      wa[n] = __builtin_amdgcn_raw_buffer_load_b128(rW1, l16, ((ft0 + a) * 28 + ks) * 1024, 0);
    } else {
      const int m = n - 56, i = m >> 1, dt = m & 1;
      int jj = j + (i >> 2); jj = jj >= 7 ? jj - 7 : jj;
      const int fs = 16 * jj + 4 * w + (i & 3);
      wb[dt][i] = __builtin_amdgcn_raw_buffer_load_b128(rW2, l16, ((2 * j + dt) * 112 + fs) * 1024, 0);
    }
  };
#pragma unroll
  for (int n = 0; n < WIN0; ++n) seqload(n);
  SB();
  {
    const int r = tid >> 3, c = tid & 7;
#pragma unroll
    for (int p = 0; p < 14; ++p) { const int row = 32 * p + r; *(LAS u32x4*)(smem + M_HT + row * 128 + ((c ^ (4 * ((row >> 1) & 1))) * 16)) = hv[p]; }
  }
  *(LAS float*)(smem + M_CS + (w * 64 + lane) * 4) = (csp[0] + csp[1]) + (csp[2] + csp[3]);
  SB();
  BAR();
  const int hh = lane >> 5, blk = (lane >> 4) & 1, q = (lane & 15) >> 2, p = lane & 3;
  const int sw = (q >> 1) & 1;
  const LAS char* b0 = smem + M_HT + (8 * hh + q) * 128 + ((2 * blk + (p >> 1) + 4 * sw) * 16) + (p & 1) * 8;
  const LAS char* b1 = smem + M_HT + (8 * hh + q) * 128 + ((2 * blk + (p >> 1) + 4 * (1 - sw)) * 16) + (p & 1) * 8;
  f32x16 acc[2][2];
#pragma unroll
  for (int a = 0; a < 2; ++a)
#pragma unroll
    for (int c = 0; c < 2; ++c)
#pragma unroll
      for (int e = 0; e < 16; ++e) acc[a][c][e] = 0.f;
  float s0 = 0.f, s1 = 0.f, q0 = 0.f, q1 = 0.f;
  const unsigned ONES = 0x3f803f80u;
  constexpr int BD = 3;
  u32x4 bf[BD + 1][2];
  auto bload = [&](int i, u32x4& o0, u32x4& o1) {
    const int go = ((((i / 7) + w) & 3) * 7 + (i % 7)) * 2048;
    o0 = tr_read2(b0 + go, b0 + go + 512);
    o1 = tr_read2(b1 + go, b1 + go + 512);
  };
  float mr[2], ri[2];
  u32x4 own[2][4];
  auto epi = [&](int a, int c, int s) {
    u32x4 fr;
    const f32x4 c0 = *(const LAS f32x4*)(smem + M_CS + (w * 64 + a * 32 + 16 * s + 4 * hh) * 4);
    const f32x4 c1 = *(const LAS f32x4*)(smem + M_CS + (w * 64 + a * 32 + 16 * s + 8 + 4 * hh) * 4);
#pragma unroll
    for (int e = 0; e < 4; ++e) {
      const int r0 = 8 * s + 2 * e;
      const float k0 = e < 2 ? c0[2 * e] : c1[2 * e - 4], k1 = e < 2 ? c0[2 * e + 1] : c1[2 * e - 3];
      const float v0 = (acc[a][c][r0] - mr[c] * k0) * ri[c], v1 = (acc[a][c][r0 + 1] - mr[c] * k1) * ri[c];
      fr[e] = pkbf(fmaxf(v0, 0.f), fmaxf(v1, 0.f));
    }
    own[c][2 * a + s] = fr;
    const int fs = 16 * j + 4 * w + 2 * a + s;
    __builtin_amdgcn_raw_buffer_store_b128(fr, rH1, l16, ((rt * 2 + c) * 112 + fs) * 1024, 16);
  };
#pragma unroll
  for (int i = 0; i < BD; ++i) bload(i, bf[i][0], bf[i][1]);
  static_for<0, 56>([&](auto T_) {
    constexpr int t = decltype(T_)::value;
    constexpr int a = t / 28;
    if constexpr (t + BD < 56) bload((t + BD) % 28, bf[(t + BD) % (BD + 1)][0], bf[(t + BD) % (BD + 1)][1]);
    const u32x4 f0 = bf[t % (BD + 1)][0], f1 = bf[t % (BD + 1)][1];
    static_for<win_tgt(t - 1), win_tgt(t)>([&](auto N_) { seqload(decltype(N_)::value); });
    SB();
    acc[a][0] = mfma32b(wa[t], f0, acc[a][0]);
    acc[a][1] = mfma32b(wa[t], f1, acc[a][1]);
    if constexpr (t < 7) {
#pragma unroll
      for (int e = 0; e < 4; ++e) { s0 = dot2bf(f0[e], ONES, s0); q0 = dot2bf(f0[e], f0[e], q0); s1 = dot2bf(f1[e], ONES, s1); q1 = dot2bf(f1[e], f1[e], q1); }
    }
    if constexpr (t == 8) {
      s0 += __shfl_xor(s0, 32); s1 += __shfl_xor(s1, 32); q0 += __shfl_xor(q0, 32); q1 += __shfl_xor(q1, 32);
      if (hh == 0) {
        *(LAS u32x2*)(smem + M_ST + (w * 64 + lane) * 8) = u32x2{__float_as_uint(s0), __float_as_uint(q0)};
        *(LAS u32x2*)(smem + M_ST + (w * 64 + 32 + lane) * 8) = u32x2{__float_as_uint(s1), __float_as_uint(q1)};
      }
    }
    if constexpr (t == 27) {
      BAR();
#pragma unroll
      for (int c = 0; c < 2; ++c) {
        float s = 0.f, qq = 0.f;
#pragma unroll
        for (int ww = 0; ww < 4; ++ww) { u32x2 v = *(const LAS u32x2*)(smem + M_ST + (ww * 64 + c * 32 + (lane & 31)) * 8); s += __uint_as_float(v[0]); qq += __uint_as_float(v[1]); }
        const float mean = s / (float)D;
        const float var = fmaxf(qq / (float)D - mean * mean, 0.f);
        float mb, sb; ln_scalars(mean, var, mb, sb);
        mr[c] = mb; ri[c] = rcp_(sb);
      }
    }
    if constexpr (t >= 30 && t < 54 && (t - 30) % 6 == 0) { constexpr int e = (t - 30) / 6; epi(0, e >> 1, e & 1); }
    SB();
  });
#pragma unroll
  for (int e = 0; e < 4; ++e) { epi(1, e >> 1, e & 1); SB(); }
#pragma unroll
  for (int n = 68; n < 84; ++n) seqload(n);
  SB();
  unsigned elo = __builtin_amdgcn_readfirstlane(ev[0]), ehi = __builtin_amdgcn_readfirstlane(ev[1]);
  asm volatile("" : "+s"(elo), "+s"(ehi));
  const u64 tag = ((u64)ehi << 32) | elo;
  asm volatile("s_waitcnt vmcnt(16)" ::: "memory");
  GAS u64* flg = (GAS u64*)(ws + WS_FLAGB) + (rt * 4 + w) * 8;
  if (lane == 0) __hip_atomic_store(flg + j, tag, __ATOMIC_RELAXED, __HIP_MEMORY_SCOPE_AGENT);
  SB();
#pragma unroll
  for (int n = 84; n < 102; ++n) seqload(n);
  SB();
  const int fl = lane < 7 ? lane : 0;
  u64 fv = __hip_atomic_load(flg + fl, __ATOMIC_RELAXED, __HIP_MEMORY_SCOPE_AGENT);
  SB();
#pragma unroll
  for (int n = 102; n < 112; ++n) seqload(n);
  SB();
#pragma unroll
  for (int a = 0; a < 2; ++a)
#pragma unroll
    for (int c = 0; c < 2; ++c)
#pragma unroll
      for (int e = 0; e < 16; ++e) acc[a][c][e] = 0.f;
#pragma unroll
  for (int i = 0; i < 4; ++i) {
    acc[0][0] = mfma32b(wb[0][i], own[0][i], acc[0][0]);
    acc[0][1] = mfma32b(wb[0][i], own[1][i], acc[0][1]);
    acc[1][0] = mfma32b(wb[1][i], own[0][i], acc[1][0]);
    acc[1][1] = mfma32b(wb[1][i], own[1][i], acc[1][1]);
  }
  SB();
  {
    for (unsigned spins = 0;; ++spins) {
      if (__all(fv == tag)) break;
      if (spins > kSpinCap) { if (lane == 0) __hip_atomic_store((GAS unsigned*)(ws + WS_TMO), 2u, __ATOMIC_RELAXED, __HIP_MEMORY_SCOPE_AGENT); break; }
      fv = __hip_atomic_load(flg + fl, __ATOMIC_RELAXED, __HIP_MEMORY_SCOPE_AGENT);
    }
  }
  asm volatile("" ::: "memory");
  u32x4 hf[2][24];
  auto h1load = [&](int i) {
    const int n = i >> 2, fsub = i & 3;
    int jj = j + n; jj = jj >= 7 ? jj - 7 : jj;
    const int fs = 16 * jj + 4 * w + fsub;
    hf[0][i - 4] = __builtin_amdgcn_raw_buffer_load_b128(rH1, l16, ((rt * 2) * 112 + fs) * 1024, 16);
    hf[1][i - 4] = __builtin_amdgcn_raw_buffer_load_b128(rH1, l16, ((rt * 2 + 1) * 112 + fs) * 1024, 16);
  };
  constexpr int HW = 8;
#pragma unroll
  for (int i = 4; i < 4 + HW; ++i) h1load(i);
  SB();
  static_for<4, 28>([&](auto I_) {
    constexpr int i = decltype(I_)::value;
    if constexpr (i + HW < 28) h1load(i + HW);
    SB();
    acc[0][0] = mfma32b(wb[0][i], hf[0][i - 4], acc[0][0]);
    acc[0][1] = mfma32b(wb[0][i], hf[1][i - 4], acc[0][1]);
    acc[1][0] = mfma32b(wb[1][i], hf[0][i - 4], acc[1][0]);
    acc[1][1] = mfma32b(wb[1][i], hf[1][i - 4], acc[1][1]);
    SB();
  });
#pragma unroll
  for (int a = 0; a < 2; ++a)
#pragma unroll
    for (int c = 0; c < 2; ++c)
#pragma unroll
      for (int g4 = 0; g4 < 4; ++g4) {
        f32x4 v = {acc[a][c][4 * g4], acc[a][c][4 * g4 + 1], acc[a][c][4 * g4 + 2], acc[a][c][4 * g4 + 3]};
        *(LAS f32x4*)(smem + M_P + (w * 64 + c * 32 + (lane & 31)) * 272 + (a * 32 + 8 * g4 + 4 * hh) * 4) = v;
      }
  BAR();
  {
    const int l = lane, c4 = w;
    float o[16];
#pragma unroll
    for (int e = 0; e < 16; ++e) o[e] = 0.f;
#pragma unroll
    for (int ww = 0; ww < 4; ++ww)
#pragma unroll
      for (int i = 0; i < 4; ++i) {
        f32x4 v = *(const LAS f32x4*)(smem + M_P + (ww * 64 + l) * 272 + (16 * c4 + 4 * i) * 4);
#pragma unroll
        for (int e = 0; e < 4; ++e) o[4 * i + e] += v[e];
      }
    unsigned pk[8];
#pragma unroll
    for (int e = 0; e < 8; ++e) {
      const int r0 = 64 * j + 16 * c4 + 2 * e, r1 = r0 + 1;
      const float h0 = bf2f(*(const LAS unsigned short*)(smem + M_HT + r0 * 128 + (((l >> 3) ^ (4 * ((r0 >> 1) & 1))) * 16) + (l & 7) * 2));
      const float h1 = bf2f(*(const LAS unsigned short*)(smem + M_HT + r1 * 128 + (((l >> 3) ^ (4 * ((r1 >> 1) & 1))) * 16) + (l & 7) * 2));
      const unsigned ob = pkbf(o[2 * e], o[2 * e + 1]);
      pk[e] = pkbf(h0 + bflo(ob), h1 + bfhi(ob));
    }
    char* op = (char*)out + ((size_t)(rt * 64 + l) * D + 64 * j + 16 * c4) * 2;
    *(u32x4*)op = u32x4{pk[0], pk[1], pk[2], pk[3]};
    *(u32x4*)(op + 16) = u32x4{pk[4], pk[5], pk[6], pk[7]};
  }
}

}

extern "C" void kernel_launch(void* const* d_in, const int* in_sizes, int n_in, void* d_out, int out_size, void* d_ws, size_t ws_size, hipStream_t stream) {
  typedef const unsigned short* cp;
  k_ssm<<<256, 512, 0, stream>>>((cp)d_in[0], (cp)d_in[1], (cp)d_in[2], (cp)d_in[3], (cp)d_in[4], (cp)d_in[5], (cp)d_in[6], (cp)d_in[7], (cp)d_in[8], (cp)d_in[10], (cp)d_in[11], (char*)d_ws);
  k_mlp<<<224, 256, 0, stream>>>((char*)d_ws, (unsigned short*)d_out);
}
```
